```python
import torch

def _softmax(x, axis=-1):
    return torch.softmax(x, dim=axis)

def _gelu(x):
    return torch.nn.functional.gelu(x, approximate='tanh')

def _silu(x):
    return torch.nn.functional.silu(x)

def _sigmoid(x):
    return torch.sigmoid(x)

def _layernorm(x, axis=-1, eps=1e-05):
    m = x.mean(dim=axis, keepdim=True)
    v = x.var(dim=axis, keepdim=True, unbiased=False)
    return (x - m) / torch.sqrt(v + eps)

def _scatter_add(tgt, idx, src, axis=0):
    return tgt.clone().index_add_(axis, idx, src)

def _segment_sum(src, seg, n):
    out = torch.zeros((n,) + src.shape[1:], dtype=src.dtype)
    return out.index_add_(0, seg, src)

def _pad(x, pw):
    flat = tuple((v for pair in reversed(pw) for v in pair))
    return torch.nn.functional.pad(x, flat)
kernel_name = 'interleave_layers__softmax_full_mha__memory_aug_recurrent_mem_tr__prompt'

def setup_inputs():

    _w = lambda a: (lambda t: t.to(torch.bfloat16) if t.is_floating_point() else t)(torch.as_tensor(a))

    def _A_setup():
        B, S, D = (1, 2048, 512)
        return {'x': _w((lambda *s: torch.randn(*s, dtype=torch.bfloat16) * 0.05)(B, S, D)), 'wqkv': _w((lambda *s: torch.randn(*s, dtype=torch.bfloat16) * 0.05)(D, 3 * D)), 'wo': _w((lambda *s: torch.randn(*s, dtype=torch.bfloat16) * 0.05)(D, D))}
    _A_in = _A_setup()

    def _B_setup():
        B, S, Dm = (1, 2048, 512)
        H, Dh = (32, 128)
        M, seg = (8, 32)
        seg = min(seg, S)
        S = S // seg * seg
        return {'x': _w((lambda *s: torch.randn(*s, dtype=torch.bfloat16) * 0.05)(B, S, Dm)), 'mem0': _w((lambda *s: torch.randn(*s, dtype=torch.bfloat16) * 0.8)(B, M, Dm)), 'Wq': _w((lambda *s: torch.randn(*s, dtype=torch.bfloat16) * 1.6)(Dm, H, Dh) * Dm ** (-0.5)), 'Wk': _w((lambda *s: torch.randn(*s, dtype=torch.bfloat16) * 1.6)(Dm, H, Dh) * Dm ** (-0.5)), 'Wv': _w((lambda *s: torch.randn(*s, dtype=torch.bfloat16) * 0.8)(Dm, H, Dh) * Dm ** (-0.5)), 'Wo': _w((lambda *s: torch.randn(*s, dtype=torch.bfloat16) * 0.8)(H, Dh, Dm) * (H * Dh) ** (-0.5)), 'seg_len': seg}
    _B_in = _B_setup()
    return {'A_x': _A_in['x'], 'A_wqkv': _A_in['wqkv'], 'A_wo': _A_in['wo'], 'B_x': _B_in['x'], 'B_mem0': _B_in['mem0'], 'B_Wq': _B_in['Wq'], 'B_Wk': _B_in['Wk'], 'B_Wv': _B_in['Wv'], 'B_Wo': _B_in['Wo'], 'B_seg_len': _B_in['seg_len']}

def reference(A_x, A_wqkv, A_wo, B_x, B_mem0, B_Wq, B_Wk, B_Wv, B_Wo, B_seg_len):

    def _A_ref(x, wqkv, wo):
        B, S, D = x.shape
        H = 32
        Dh = D // H
        qkv = (lambda x, *d: x.permute(*d))((x @ wqkv).reshape(B, S, 3, H, Dh), 2, 0, 3, 1, 4)
        q, k, v = (qkv[0], qkv[1], qkv[2])
        scale = Dh ** (-0.5)
        scores = torch.einsum('bhqd,bhkd->bhqk', q, k) * scale
        p = _softmax(scores, axis=-1)
        o = torch.einsum('bhqk,bhkd->bhqd', p, v)
        return (lambda x, *d: x.permute(*d))(o, 0, 2, 1, 3).reshape(B, S, D) @ wo

    def _B_ref(x, mem0, Wq, Wk, Wv, Wo, seg_len):
        B, S, Dm = x.shape
        H, Dh = (Wq.shape[1], Wq.shape[2])
        M = mem0.shape[1]
        scale = Dh ** (-0.5)
        n_seg = S // seg_len
        L = M + seg_len
        idx = torch.arange(L)
        mask = (idx[:, None] < M) | (idx[:, None] >= idx[None, :])
        mem = mem0
        outs = []
        for s in range(n_seg):
            xs = x[:, s * seg_len:(s + 1) * seg_len, :].clone()
            h = torch.concatenate([mem, xs], axis=1)
            q = torch.einsum('btd,dhe->bhte', h, Wq)
            k = torch.einsum('btd,dhe->bhte', h, Wk)
            v = torch.einsum('btd,dhe->bhte', h, Wv)
            scores = torch.einsum('bhqe,bhke->bhqk', q, k) * scale
            scores = torch.where(mask[None, None], scores, -1000000000.0)
            p = _softmax(scores, axis=-1)
            o = torch.einsum('bhqk,bhke->bhqe', p, v)
            o = torch.einsum('bhte,hed->btd', o, Wo)
            mem = o[:, :M, :].clone()
            outs.append(o[:, M:, :])
        return torch.concatenate(outs, axis=1)
    x = A_x
    x = x + _A_ref(_layernorm(x, axis=-1), A_wqkv, A_wo)
    x = x + _B_ref(_layernorm(x, axis=-1), B_mem0, B_Wq, B_Wk, B_Wv, B_Wo, B_seg_len)
    x = x + _A_ref(_layernorm(x, axis=-1), A_wqkv, A_wo)
    x = x + _B_ref(_layernorm(x, axis=-1), B_mem0, B_Wq, B_Wk, B_Wv, B_Wo, B_seg_len)
    return x
```

```cpp
#include <hip/hip_runtime.h>
#include <hip/hip_bf16.h>
#include <stdint.h>

typedef __bf16 bf16x2 __attribute__((ext_vector_type(2)));
typedef __bf16 bf16x4 __attribute__((ext_vector_type(4)));
typedef __bf16 bf16x8 __attribute__((ext_vector_type(8)));
typedef __bf16 bf16x16 __attribute__((ext_vector_type(16)));
typedef float f32x2 __attribute__((ext_vector_type(2)));
typedef float f32x4 __attribute__((ext_vector_type(4)));
typedef float f32x16 __attribute__((ext_vector_type(16)));
typedef unsigned u32x2 __attribute__((ext_vector_type(2)));
typedef unsigned u32x4 __attribute__((ext_vector_type(4)));
#define LAS __attribute__((address_space(3)))
#define GAS __attribute__((address_space(1)))
typedef LAS char* ldsp;
#define DEV __device__ __forceinline__
#define SBAR() __builtin_amdgcn_sched_barrier(0)
#define PINV(x) asm volatile("" : "+v"(x))
#define PINA(x) asm volatile("" : "+a"(x))
#define MEMFENCE() asm volatile("" ::: "memory")
#define WAIT_VM0() asm volatile("s_waitcnt vmcnt(0)" ::: "memory")
#define WAIT_LGKM0() asm volatile("s_waitcnt lgkmcnt(0)" ::: "memory")
#define AUX_SC1 16

DEV unsigned pk2(float a, float b) { bf16x2 r = {(__bf16)a, (__bf16)b}; return __builtin_bit_cast(unsigned, r); }
DEV bf16x8 asbf8(u32x4 v) { return __builtin_bit_cast(bf16x8, v); }
DEV u32x4 asu4(bf16x8 v) { return __builtin_bit_cast(u32x4, v); }
DEV f32x4 mfma16(bf16x8 a, bf16x8 b, f32x4 c) { return __builtin_amdgcn_mfma_f32_16x16x32_bf16(a, b, c, 0, 0, 0); }
DEV f32x16 mfma32(bf16x8 a, bf16x8 b, f32x16 c) { return __builtin_amdgcn_mfma_f32_32x32x16_bf16(a, b, c, 0, 0, 0); }
DEV f32x4 smf(bf16x8 a, bf16x16 b, f32x4 c, int idx) { return __builtin_amdgcn_smfmac_f32_16x16x64_bf16(a, b, c, idx, 0, 0); }
DEV bf16x4 trread(ldsp p) { return __builtin_amdgcn_ds_read_tr16_b64_v4bf16((LAS bf16x4*)p); }
DEV bf16x8 cat4(bf16x4 a, bf16x4 b) { return __builtin_shufflevector(a, b, 0, 1, 2, 3, 4, 5, 6, 7); }
DEV bf16x16 cat8(bf16x8 a, bf16x8 b) { return __builtin_shufflevector(a, b, 0, 1, 2, 3, 4, 5, 6, 7, 8, 9, 10, 11, 12, 13, 14, 15); }
DEV u32x4 ldsr128(ldsp p) { return *(LAS u32x4*)p; }
DEV void ldsw128(ldsp p, u32x4 v) { *(LAS u32x4*)p = v; }
DEV void ldsw64(ldsp p, u32x2 v) { *(LAS u32x2*)p = v; }
DEV float ror8(float v) { return __builtin_bit_cast(float, __builtin_amdgcn_update_dpp(0, __builtin_bit_cast(int, v), 0x128, 0xf, 0xf, false)); }
DEV float ror4(float v) { return __builtin_bit_cast(float, __builtin_amdgcn_update_dpp(0, __builtin_bit_cast(int, v), 0x124, 0xf, 0xf, false)); }
DEV float xor16(float v) { unsigned u = __float_as_uint(v); auto r = __builtin_amdgcn_permlane16_swap(u, u, false, false);
    return __uint_as_float(r[0]) , __uint_as_float((threadIdx.x & 16) ? r[0] : r[1]); }
DEV float max_xor16(float v) { unsigned u = __float_as_uint(v); auto r = __builtin_amdgcn_permlane16_swap(u, u, false, false); return fmaxf(__uint_as_float(r[0]), __uint_as_float(r[1])); }
DEV float max_xor32(float v) { unsigned u = __float_as_uint(v); auto r = __builtin_amdgcn_permlane32_swap(u, u, false, false); return fmaxf(__uint_as_float(r[0]), __uint_as_float(r[1])); }
DEV float sum_xor16(float v) { unsigned u = __float_as_uint(v); auto r = __builtin_amdgcn_permlane16_swap(u, u, false, false); return __uint_as_float(r[0]) + __uint_as_float(r[1]); }
DEV float sum_xor32(float v) { unsigned u = __float_as_uint(v); auto r = __builtin_amdgcn_permlane32_swap(u, u, false, false); return __uint_as_float(r[0]) + __uint_as_float(r[1]); }

typedef __amdgpu_buffer_rsrc_t rsrc_t;
DEV rsrc_t mkrsrc(const void* p, unsigned bytes) { return __builtin_amdgcn_make_buffer_rsrc((void*)p, 0, bytes, 0x00020000); }
DEV u32x4 bld16(rsrc_t r, unsigned off, int aux_is_sc1) { return __builtin_bit_cast(u32x4, aux_is_sc1 ? __builtin_amdgcn_raw_buffer_load_b128(r, off, 0, AUX_SC1) : __builtin_amdgcn_raw_buffer_load_b128(r, off, 0, 0)); }
DEV u32x4 bld16s(rsrc_t r, unsigned off) { return __builtin_bit_cast(u32x4, __builtin_amdgcn_raw_buffer_load_b128(r, off, 0, AUX_SC1)); }
DEV u32x4 bld16p(rsrc_t r, unsigned off) { return __builtin_bit_cast(u32x4, __builtin_amdgcn_raw_buffer_load_b128(r, off, 0, 0)); }
DEV u32x2 bld8s(rsrc_t r, unsigned off) { return __builtin_bit_cast(u32x2, __builtin_amdgcn_raw_buffer_load_b64(r, off, 0, AUX_SC1)); }
DEV unsigned bld4s(rsrc_t r, unsigned off) { return __builtin_amdgcn_raw_buffer_load_b32(r, off, 0, AUX_SC1); }
DEV void bst16s(rsrc_t r, unsigned off, u32x4 v) { __builtin_amdgcn_raw_buffer_store_b128(v, r, off, 0, AUX_SC1); }
DEV void bst8s(rsrc_t r, unsigned off, u32x2 v) { __builtin_amdgcn_raw_buffer_store_b64(v, r, off, 0, AUX_SC1); }
DEV void bst4s(rsrc_t r, unsigned off, unsigned v) { __builtin_amdgcn_raw_buffer_store_b32(v, r, off, 0, AUX_SC1); }
DEV void bst16p(rsrc_t r, unsigned off, u32x4 v) { __builtin_amdgcn_raw_buffer_store_b128(v, r, off, 0, 0); }
DEV void bst8p(rsrc_t r, unsigned off, u32x2 v) { __builtin_amdgcn_raw_buffer_store_b64(v, r, off, 0, 0); }
DEV bool notsent(u32x4 v) { unsigned m = max(max(v.x, v.y), max(v.z, v.w)); return m != 0xFFFFFFFFu; }
DEV unsigned san(unsigned x) { return min(x, 0xFFFFFFFEu); }

#define MF0(d, a, b) asm volatile("s_nop 1\n\tv_mfma_f32_16x16x32_bf16 %0, %1, %2, 0" : "=&v"(d) : "v"(a), "v"(b))
#define MF0N(d, a, b) asm volatile("v_mfma_f32_16x16x32_bf16 %0, %1, %2, 0" : "=&v"(d) : "v"(a), "v"(b))
#define MFA(d, a, b) asm volatile("v_mfma_f32_16x16x32_bf16 %0, %1, %2, %0" : "+v"(d) : "v"(a), "v"(b))
#define MFA1(d, a, b) asm volatile("s_nop 1\n\tv_mfma_f32_16x16x32_bf16 %0, %1, %2, %0" : "+v"(d) : "v"(a), "v"(b))
#define MFA_AV(d, a, b) asm volatile("v_mfma_f32_16x16x32_bf16 %0, %1, %2, %0" : "+v"(d) : "a"(a), "v"(b))
#define MFA_VA(d, a, b) asm volatile("v_mfma_f32_16x16x32_bf16 %0, %1, %2, %0" : "+v"(d) : "v"(a), "a"(b))
#define MF0_AV(d, a, b) asm volatile("v_mfma_f32_16x16x32_bf16 %0, %1, %2, 0" : "=&v"(d) : "a"(a), "v"(b))
#define MF0_VA(d, a, b) asm volatile("v_mfma_f32_16x16x32_bf16 %0, %1, %2, 0" : "=&v"(d) : "v"(a), "a"(b))
#define NOPS(n) asm volatile("s_nop " #n)

#define MF16_0(d, a, b) asm volatile("v_mfma_f32_16x16x16_bf16 %0, %1, %2, 0" : "=&v"(d) : "v"(a), "v"(b))
#define MF16_A(d, a, b) asm volatile("v_mfma_f32_16x16x16_bf16 %0, %1, %2, %0" : "+v"(d) : "v"(a), "v"(b))
DEV float vmax3(float a, float b, float c) { float r; asm("v_max3_f32 %0, %1, %2, %3" : "=v"(r) : "v"(a), "v"(b), "v"(c)); return r; }
DEV float vmax2(float a, float b) { float r; asm("v_max_f32 %0, %1, %2" : "=v"(r) : "v"(a), "v"(b)); return r; }
DEV float red8_max(float x) {
    float t;
    asm volatile("s_nop 1\n\tv_max_f32_dpp %0, %0, %0 row_ror:8 row_mask:0xf bank_mask:0xf\n\t"
                 "v_mov_b32 %1, %0\n\ts_nop 1\n\tv_permlane16_swap_b32 %0, %1\n\tv_max_f32 %0, %0, %1\n\t"
                 "v_mov_b32 %1, %0\n\ts_nop 1\n\tv_permlane32_swap_b32 %0, %1\n\tv_max_f32 %0, %0, %1" : "+v"(x), "=&v"(t));
    return x;
}
DEV float red8_sum(float x) {
    float t;
    asm volatile("s_nop 1\n\tv_add_f32_dpp %0, %0, %0 row_ror:8 row_mask:0xf bank_mask:0xf\n\t"
                 "v_mov_b32 %1, %0\n\ts_nop 1\n\tv_permlane16_swap_b32 %0, %1\n\tv_add_f32 %0, %0, %1\n\t"
                 "v_mov_b32 %1, %0\n\ts_nop 1\n\tv_permlane32_swap_b32 %0, %1\n\tv_add_f32 %0, %0, %1" : "+v"(x), "=&v"(t));
    return x;
}

#define TLMARK(wsp, id) do {} while (0)

#define RH_ASM \
    "v_mov_b32_e32 v178, -1\n\t" \
    "v_mov_b32_e32 v179, -1\n\t" \
    "v_mov_b32_e32 v243, s47\n\t" \
    "v_mov_b64_e32 v[32:33], 0\n\t" \
    "v_mov_b64_e32 v[34:35], 0\n\t" \
    "v_mov_b64_e32 v[36:37], 0\n\t" \
    "v_mov_b64_e32 v[38:39], 0\n\t" \
    "v_mov_b64_e32 v[40:41], 0\n\t" \
    "v_mov_b64_e32 v[42:43], 0\n\t" \
    "v_mov_b64_e32 v[44:45], 0\n\t" \
    "v_mov_b64_e32 v[46:47], 0\n\t" \
    "v_mov_b64_e32 v[48:49], 0\n\t" \
    "v_mov_b64_e32 v[50:51], 0\n\t" \
    "v_mov_b64_e32 v[52:53], 0\n\t" \
    "v_mov_b64_e32 v[54:55], 0\n\t" \
    "ds_read_b128 v[180:183], v233 offset:0\n\t" \
    "ds_read_b128 v[184:187], v233 offset:1024\n\t" \
    "ds_read_b128 v[188:191], v233 offset:8192\n\t" \
    "ds_read_b128 v[192:195], v233 offset:9216\n\t" \
    "ds_read_b128 v[196:199], v233 offset:16384\n\t" \
    "ds_read_b128 v[200:203], v233 offset:17408\n\t" \
    "ds_read_b128 v[204:207], v233 offset:24576\n\t" \
    "ds_read_b128 v[208:211], v233 offset:25600\n\t" \
    "s_waitcnt lgkmcnt(0)\n\t" \
    "s_mov_b32 s44, 0\n\t" \
    "s_mov_b32 s64, 0\n\t" \
    "s_and_b32 s66, s44, 1\n\t" \
    "s_lshl_b32 s56, s66, 19\n\t" \
    "s_xor_b32 s57, s56, 524288\n\t" \
    "s_add_u32 s69, s56, 0x10000\n\t" \
    "s_add_u32 s66, s44, 1\n\t" \
    "s_and_b32 s67, s66, 3\n\t" \
    "s_lshl_b32 s58, s67, 14\n\t" \
    "s_add_u32 s67, s44, 3\n\t" \
    "s_and_b32 s67, s67, 3\n\t" \
    "s_lshl_b32 s59, s67, 14\n\t" \
    "s_lshl_b32 s66, s66, 18\n\t" \
    "s_add_u32 s60, s66, 25165824\n\t" \
    "s_add_u32 s61, s66, 41943040\n\t" \
    "s_lshl_b32 s62, s44, 17\n\t" \
    "LRH_loop_%=:\n\t" \
    "ds_read_b64_tr_b16 v[0:1], v232 offset:0\n\t" \
    "ds_read_b64_tr_b16 v[2:3], v232 offset:128\n\t" \
    "ds_read_b64_tr_b16 v[4:5], v232 offset:1536\n\t" \
    "ds_read_b64_tr_b16 v[6:7], v232 offset:1664\n\t" \
    "ds_read_b64_tr_b16 v[8:9], v232 offset:3072\n\t" \
    "ds_read_b64_tr_b16 v[10:11], v232 offset:3200\n\t" \
    "ds_read_b64_tr_b16 v[12:13], v232 offset:4608\n\t" \
    "ds_read_b64_tr_b16 v[14:15], v232 offset:4736\n\t" \
    "s_waitcnt lgkmcnt(6)\n\t" \
    "v_smfmac_f32_16x16x64_bf16 v[32:35], v[0:3], a[0:7], v250\n\t" \
    "ds_read_b64_tr_b16 v[16:17], v232 offset:6144\n\t" \
    "v_smfmac_f32_16x16x64_bf16 v[36:39], v[0:3], a[64:71], v250\n\t" \
    "ds_read_b64_tr_b16 v[18:19], v232 offset:6272\n\t" \
    "s_waitcnt lgkmcnt(6)\n\t" \
    "v_smfmac_f32_16x16x64_bf16 v[32:35], v[4:7], a[8:15], v250\n\t" \
    "ds_read_b64_tr_b16 v[20:21], v232 offset:7680\n\t" \
    "v_smfmac_f32_16x16x64_bf16 v[36:39], v[4:7], a[72:79], v250\n\t" \
    "ds_read_b64_tr_b16 v[22:23], v232 offset:7808\n\t" \
    "s_waitcnt lgkmcnt(6)\n\t" \
    "v_smfmac_f32_16x16x64_bf16 v[32:35], v[8:11], a[16:23], v250\n\t" \
    "ds_read_b64_tr_b16 v[24:25], v232 offset:9216\n\t" \
    "v_smfmac_f32_16x16x64_bf16 v[36:39], v[8:11], a[80:87], v250\n\t" \
    "ds_read_b64_tr_b16 v[26:27], v232 offset:9344\n\t" \
    "s_waitcnt lgkmcnt(6)\n\t" \
    "v_smfmac_f32_16x16x64_bf16 v[32:35], v[12:15], a[24:31], v250\n\t" \
    "ds_read_b64_tr_b16 v[28:29], v232 offset:10752\n\t" \
    "v_smfmac_f32_16x16x64_bf16 v[36:39], v[12:15], a[88:95], v250\n\t" \
    "ds_read_b64_tr_b16 v[30:31], v232 offset:10880\n\t" \
    "s_waitcnt lgkmcnt(6)\n\t" \
    "v_smfmac_f32_16x16x64_bf16 v[32:35], v[16:19], a[32:39], v250\n\t" \
    "buffer_store_dwordx2 v[178:179], v241, s[40:43], s57 offen sc1\n\t" \
    "v_smfmac_f32_16x16x64_bf16 v[36:39], v[16:19], a[96:103], v250\n\t" \
    "v_mov_b32_e32 v251, s44\n\t" \
    "s_waitcnt lgkmcnt(4)\n\t" \
    "v_smfmac_f32_16x16x64_bf16 v[32:35], v[20:23], a[40:47], v250\n\t" \
    "buffer_store_dword v251, v249, s[40:43], 0 offen sc1\n\t" \
    "v_smfmac_f32_16x16x64_bf16 v[36:39], v[20:23], a[104:111], v250\n\t" \
    "v_readfirstlane_b32 s47, v243\n\t" \
    "s_waitcnt lgkmcnt(2)\n\t" \
    "v_smfmac_f32_16x16x64_bf16 v[32:35], v[24:27], a[48:55], v250\n\t" \
    "s_add_u32 s66, s44, 2\n\t" \
    "s_cmp_lt_u32 s47, s66\n\t" \
    "v_smfmac_f32_16x16x64_bf16 v[36:39], v[24:27], a[112:119], v250\n\t" \
    "s_cbranch_scc1 LRH_h1poll_%=\n\t" \
    "LRH_h1back_%=:\n\t" \
    "buffer_load_dword v243, off, s[40:43], s48 sc1\n\t" \
    "s_waitcnt lgkmcnt(0)\n\t" \
    "v_smfmac_f32_16x16x64_bf16 v[32:35], v[28:31], a[56:63], v250\n\t" \
    "v_smfmac_f32_16x16x64_bf16 v[36:39], v[28:31], a[120:127], v250\n\t" \
    "v_smfmac_f32_16x16x64_bf16 v[40:43], v[0:3], a[128:135], v250\n\t" \
    "v_smfmac_f32_16x16x64_bf16 v[44:47], v[0:3], a[192:199], v250\n\t" \
    "v_smfmac_f32_16x16x64_bf16 v[40:43], v[4:7], a[136:143], v250\n\t" \
    "v_mul_f32_e32 v32, s72, v32\n\t" \
    "v_mul_f32_e32 v33, s72, v33\n\t" \
    "v_smfmac_f32_16x16x64_bf16 v[44:47], v[4:7], a[200:207], v250\n\t" \
    "v_mul_f32_e32 v34, s72, v34\n\t" \
    "v_mul_f32_e32 v35, s72, v35\n\t" \
    "v_smfmac_f32_16x16x64_bf16 v[40:43], v[8:11], a[144:151], v250\n\t" \
    "v_mul_f32_e32 v36, s72, v36\n\t" \
    "v_mul_f32_e32 v37, s72, v37\n\t" \
    "v_cvt_pk_bf16_f32 v212, v32, v33\n\t" \
    "v_smfmac_f32_16x16x64_bf16 v[44:47], v[8:11], a[208:215], v250\n\t" \
    "v_mul_f32_e32 v38, s72, v38\n\t" \
    "v_mul_f32_e32 v39, s72, v39\n\t" \
    "v_cvt_pk_bf16_f32 v213, v34, v35\n\t" \
    "v_smfmac_f32_16x16x64_bf16 v[40:43], v[12:15], a[152:159], v250\n\t" \
    "v_cvt_pk_bf16_f32 v214, v36, v37\n\t" \
    "v_cvt_pk_bf16_f32 v215, v38, v39\n\t" \
    "v_smfmac_f32_16x16x64_bf16 v[44:47], v[12:15], a[216:223], v250\n\t" \
    "v_mfma_f32_16x16x32_bf16 v[32:35], v[212:215], v[170:173], 0\n\t" \
    "v_mfma_f32_16x16x32_bf16 v[36:39], v[212:215], v[168:171], 0\n\t" \
    "v_smfmac_f32_16x16x64_bf16 v[40:43], v[16:19], a[160:167], v250\n\t" \
    "v_smfmac_f32_16x16x64_bf16 v[44:47], v[16:19], a[224:231], v250\n\t" \
    "v_smfmac_f32_16x16x64_bf16 v[40:43], v[20:23], a[168:175], v250\n\t" \
    "v_cvt_pk_bf16_f32 v216, v32, v33\n\t" \
    "v_cvt_pk_bf16_f32 v217, v34, v35\n\t" \
    "v_cvt_pk_bf16_f32 v218, v36, v37\n\t" \
    "v_smfmac_f32_16x16x64_bf16 v[44:47], v[20:23], a[232:239], v250\n\t" \
    "v_cvt_pk_bf16_f32 v219, v38, v39\n\t" \
    "v_smfmac_f32_16x16x64_bf16 v[40:43], v[24:27], a[176:183], v250\n\t" \
    "v_mfma_f32_16x16x32_bf16 v[32:35], v[152:155], v[216:219], 0\n\t" \
    "v_mfma_f32_16x16x32_bf16 v[36:39], v[156:159], v[216:219], 0\n\t" \
    "v_smfmac_f32_16x16x64_bf16 v[44:47], v[24:27], a[240:247], v250\n\t" \
    "buffer_load_dwordx4 v[152:155], v246, s[40:43], s60 offen offset:0 sc1\n\t" \
    "v_smfmac_f32_16x16x64_bf16 v[40:43], v[28:31], a[184:191], v250\n\t" \
    "buffer_load_dwordx4 v[156:159], v246, s[40:43], s60 offen offset:1024 sc1\n\t" \
    "v_smfmac_f32_16x16x64_bf16 v[44:47], v[28:31], a[248:255], v250\n\t" \
    "v_cndmask_b32_e64 v224, v32, v36, s[50:51]\n\t" \
    "v_cndmask_b32_e64 v225, v33, v37, s[50:51]\n\t" \
    "v_cndmask_b32_e64 v226, v34, v38, s[50:51]\n\t" \
    "v_cndmask_b32_e64 v227, v35, v39, s[50:51]\n\t" \
    "ds_write_b128 v234, v[224:227]\n\t" \
    "v_cvt_pk_bf16_f32 v212, v40, v41\n\t" \
    "v_cvt_pk_bf16_f32 v213, v42, v43\n\t" \
    "s_nop 0\n\t" \
    "v_cvt_pk_bf16_f32 v214, v44, v45\n\t" \
    "v_cvt_pk_bf16_f32 v215, v46, v47\n\t" \
    "s_nop 1\n\t" \
    "v_mfma_f32_16x16x32_bf16 v[40:43], v[212:215], v[170:173], 0\n\t" \
    "v_mfma_f32_16x16x32_bf16 v[44:47], v[212:215], v[168:171], 0\n\t" \
    "v_smfmac_f32_16x16x64_bf16 v[48:51], v[0:3], v[180:187], v250\n\t" \
    "v_smfmac_f32_16x16x64_bf16 v[52:55], v[0:3], v[188:195], v250\n\t" \
    "v_cvt_pk_bf16_f32 v220, v40, v41\n\t" \
    "v_cvt_pk_bf16_f32 v221, v42, v43\n\t" \
    "v_cvt_pk_bf16_f32 v222, v44, v45\n\t" \
    "v_cvt_pk_bf16_f32 v223, v46, v47\n\t" \
    "s_nop 1\n\t" \
    "v_mfma_f32_16x16x32_bf16 v[228:231], v[220:223], v[216:219], 0\n\t" \
    "v_smfmac_f32_16x16x64_bf16 v[48:51], v[4:7], v[196:203], v250\n\t" \
    "v_smfmac_f32_16x16x64_bf16 v[52:55], v[4:7], v[204:211], v250\n\t" \
    "buffer_store_dwordx4 v[220:223], v247, s[40:43], s62 offen sc1\n\t" \
    "s_nop 0\n\t" \
    "ds_write_b128 v234, v[228:231] offset:1024\n\t" \
    "s_waitcnt lgkmcnt(0)\n\t" \
    "s_barrier\n\t" \
    "ds_read_b128 v[32:35], v235\n\t" \
    "ds_read_b128 v[36:39], v236\n\t" \
    "ds_read_b128 v[40:43], v237\n\t" \
    "ds_read_b128 v[44:47], v235 offset:1024\n\t" \
    "ds_read_b128 v[212:215], v236 offset:1024\n\t" \
    "ds_read_b128 v[216:219], v237 offset:1024\n\t" \
    "v_smfmac_f32_16x16x64_bf16 v[48:51], v[8:11], v[56:63], v250\n\t" \
    "v_mov_b64_e32 v[0:1], 0\n\t" \
    "v_mov_b64_e32 v[2:3], 0\n\t" \
    "v_smfmac_f32_16x16x64_bf16 v[52:55], v[8:11], v[64:71], v250\n\t" \
    "v_mov_b64_e32 v[4:5], 0\n\t" \
    "v_mov_b64_e32 v[6:7], 0\n\t" \
    "v_smfmac_f32_16x16x64_bf16 v[48:51], v[12:15], v[72:79], v250\n\t" \
    "v_smfmac_f32_16x16x64_bf16 v[52:55], v[12:15], v[80:87], v250\n\t" \
    "v_smfmac_f32_16x16x64_bf16 v[48:51], v[16:19], v[88:95], v250\n\t" \
    "v_smfmac_f32_16x16x64_bf16 v[52:55], v[16:19], v[96:103], v250\n\t" \
    "v_smfmac_f32_16x16x64_bf16 v[48:51], v[20:23], v[104:111], v250\n\t" \
    "s_waitcnt lgkmcnt(5)\n\t" \
    "v_add_f32_e32 v224, v224, v32\n\t" \
    "v_add_f32_e32 v225, v225, v33\n\t" \
    "v_add_f32_e32 v226, v226, v34\n\t" \
    "v_add_f32_e32 v227, v227, v35\n\t" \
    "s_waitcnt lgkmcnt(3)\n\t" \
    "v_add_f32_e32 v36, v36, v40\n\t" \
    "v_add_f32_e32 v37, v37, v41\n\t" \
    "v_add_f32_e32 v38, v38, v42\n\t" \
    "v_add_f32_e32 v39, v39, v43\n\t" \
    "v_smfmac_f32_16x16x64_bf16 v[52:55], v[20:23], v[112:119], v250\n\t" \
    "v_add_f32_e32 v224, v224, v36\n\t" \
    "v_add_f32_e32 v225, v225, v37\n\t" \
    "v_add_f32_e32 v226, v226, v38\n\t" \
    "v_add_f32_e32 v227, v227, v39\n\t" \
    "v_exp_f32_e32 v220, v224\n\t" \
    "v_exp_f32_e32 v221, v225\n\t" \
    "v_exp_f32_e32 v222, v226\n\t" \
    "v_exp_f32_e32 v223, v227\n\t" \
    "v_smfmac_f32_16x16x64_bf16 v[48:51], v[24:27], v[120:127], v250\n\t" \
    "s_waitcnt lgkmcnt(2)\n\t" \
    "v_add_f32_e32 v228, v228, v44\n\t" \
    "v_add_f32_e32 v229, v229, v45\n\t" \
    "v_add_f32_e32 v230, v230, v46\n\t" \
    "v_add_f32_e32 v231, v231, v47\n\t" \
    "s_waitcnt lgkmcnt(0)\n\t" \
    "v_add_f32_e32 v212, v212, v216\n\t" \
    "v_add_f32_e32 v213, v213, v217\n\t" \
    "v_add_f32_e32 v214, v214, v218\n\t" \
    "v_add_f32_e32 v215, v215, v219\n\t" \
    "v_smfmac_f32_16x16x64_bf16 v[52:55], v[24:27], v[128:135], v250\n\t" \
    "v_add_f32_e32 v228, v228, v212\n\t" \
    "v_add_f32_e32 v229, v229, v213\n\t" \
    "v_add_f32_e32 v230, v230, v214\n\t" \
    "v_add_f32_e32 v231, v231, v215\n\t" \
    "v_cvt_pk_bf16_f32 v212, v220, v221\n\t" \
    "v_cvt_pk_bf16_f32 v213, v222, v223\n\t" \
    "ds_read_b128 v[180:183], v233 offset:32768\n\t" \
    "ds_read_b128 v[184:187], v233 offset:33792\n\t" \
    "ds_read_b128 v[188:191], v233 offset:40960\n\t" \
    "ds_read_b128 v[192:195], v233 offset:41984\n\t" \
    "ds_read_b128 v[196:199], v233 offset:49152\n\t" \
    "ds_read_b128 v[200:203], v233 offset:50176\n\t" \
    "ds_read_b128 v[204:207], v233 offset:57344\n\t" \
    "ds_read_b128 v[208:211], v233 offset:58368\n\t" \
    "v_mfma_f32_16x16x16_bf16 v[32:35], v[160:161], v[212:213], 0\n\t" \
    "v_exp_f32_e32 v216, v228\n\t" \
    "v_mfma_f32_16x16x16_bf16 v[44:47], v[166:167], v[212:213], 0\n\t" \
    "v_exp_f32_e32 v217, v229\n\t" \
    "v_mfma_f32_16x16x16_bf16 v[40:43], v[164:165], v[212:213], 0\n\t" \
    "v_exp_f32_e32 v218, v230\n\t" \
    "v_mfma_f32_16x16x16_bf16 v[36:39], v[162:163], v[212:213], 0\n\t" \
    "v_exp_f32_e32 v219, v231\n\t" \
    "v_smfmac_f32_16x16x64_bf16 v[48:51], v[28:31], v[136:143], v250\n\t" \
    "v_smfmac_f32_16x16x64_bf16 v[52:55], v[28:31], v[144:151], v250\n\t" \
    "s_nop 4\n\t" \
    "v_cvt_pk_bf16_f32 v8, v48, v49\n\t" \
    "v_cvt_pk_bf16_f32 v9, v50, v51\n\t" \
    "s_nop 0\n\t" \
    "v_cvt_pk_bf16_f32 v10, v52, v53\n\t" \
    "v_cvt_pk_bf16_f32 v11, v54, v55\n\t" \
    "v_cvt_pk_bf16_f32 v214, v216, v217\n\t" \
    "v_cvt_pk_bf16_f32 v215, v218, v219\n\t" \
    "v_add_f32_e32 v251, v220, v221\n\t" \
    "v_add_f32_e32 v252, v222, v223\n\t" \
    "v_add_f32_e32 v253, v216, v217\n\t" \
    "v_add_f32_e32 v254, v218, v219\n\t" \
    "v_add_f32_e32 v251, v251, v252\n\t" \
    "v_add_f32_e32 v253, v253, v254\n\t" \
    "v_cndmask_b32_e64 v253, 0, v253, s[54:55]\n\t" \
    "v_mfma_f32_16x16x16_bf16 v[32:35], v[8:9], v[214:215], v[32:35]\n\t" \
    "v_mfma_f32_16x16x16_bf16 v[40:43], v[10:11], v[214:215], v[40:43]\n\t" \
    "v_add_f32_e32 v251, v251, v253\n\t" \
    "v_max3_f32 v252, |v224|, |v225|, |v226|\n\t" \
    "v_max3_f32 v253, |v227|, |v228|, |v229|\n\t" \
    "v_add_f32_dpp v251, v251, v251 row_ror:8 row_mask:0xf bank_mask:0xf\n\t" \
    "v_max3_f32 v252, |v230|, |v231|, v252\n\t" \
    "v_max_f32_e32 v252, v252, v253\n\t" \
    "v_cmp_lt_f32_e32 vcc, s74, v252\n\t" \
    "v_mov_b32_e32 v254, v251\n\t" \
    "s_nop 1\n\t" \
    "v_permlane16_swap_b32_e32 v251, v254\n\t" \
    "v_add_f32_e32 v251, v251, v254\n\t" \
    "v_mov_b32_e32 v254, v251\n\t" \
    "s_nop 1\n\t" \
    "v_permlane32_swap_b32_e32 v251, v254\n\t" \
    "v_add_f32_e32 v251, v251, v254\n\t" \
    "v_rcp_f32_e32 v251, v251\n\t" \
    "s_nop 0\n\t" \
    "s_cbranch_vccnz LRH_slow_%=\n\t" \
    "LRH_join_%=:\n\t" \
    "buffer_load_dwordx4 v[160:163], v246, s[40:43], s61 offen offset:0 sc1\n\t" \
    "buffer_load_dwordx4 v[164:167], v246, s[40:43], s61 offen offset:1024 sc1\n\t" \
    "buffer_store_dwordx4 v[8:11], v248, s[40:43], s62 offen sc1\n\t" \
    "v_cndmask_b32_e64 v220, v40, v36, s[50:51]\n\t" \
    "v_cndmask_b32_e64 v221, v41, v37, s[50:51]\n\t" \
    "v_cndmask_b32_e64 v222, v42, v38, s[50:51]\n\t" \
    "v_cndmask_b32_e64 v223, v43, v39, s[50:51]\n\t" \
    "v_cndmask_b32_e64 v212, v32, v44, s[50:51]\n\t" \
    "v_cndmask_b32_e64 v213, v33, v45, s[50:51]\n\t" \
    "v_cndmask_b32_e64 v214, v34, v46, s[50:51]\n\t" \
    "v_cndmask_b32_e64 v215, v35, v47, s[50:51]\n\t" \
    "v_add_f32_dpp v212, v220, v212 row_ror:8 row_mask:0xf bank_mask:0xf\n\t" \
    "v_add_f32_dpp v213, v221, v213 row_ror:8 row_mask:0xf bank_mask:0xf\n\t" \
    "v_add_f32_dpp v214, v222, v214 row_ror:8 row_mask:0xf bank_mask:0xf\n\t" \
    "v_add_f32_dpp v215, v223, v215 row_ror:8 row_mask:0xf bank_mask:0xf\n\t" \
    "v_mul_f32_e32 v212, v251, v212\n\t" \
    "v_mul_f32_e32 v213, v251, v213\n\t" \
    "v_mul_f32_e32 v214, v251, v214\n\t" \
    "v_mul_f32_e32 v215, v251, v215\n\t" \
    "v_cvt_pk_bf16_f32 v220, v212, v213\n\t" \
    "v_cvt_pk_bf16_f32 v221, v214, v215\n\t" \
    "ds_write_b64 v238, v[220:221]\n\t" \
    "s_waitcnt lgkmcnt(0)\n\t" \
    "s_barrier\n\t" \
    "ds_read_b128 v[8:11], v239\n\t" \
    "ds_read_b128 v[12:15], v239 offset:128\n\t" \
    "s_waitcnt lgkmcnt(1)\n\t" \
    "v_smfmac_f32_16x16x64_bf16 v[0:3], v[8:11], v[180:187], v250\n\t" \
    "v_smfmac_f32_16x16x64_bf16 v[4:7], v[8:11], v[196:203], v250\n\t" \
    "s_waitcnt lgkmcnt(0)\n\t" \
    "v_smfmac_f32_16x16x64_bf16 v[0:3], v[12:15], v[188:195], v250\n\t" \
    "v_smfmac_f32_16x16x64_bf16 v[4:7], v[12:15], v[204:211], v250\n\t" \
    "s_waitcnt vmcnt(7)\n\t" \
    "s_nop 6\n\t" \
    "v_permlane32_swap_b32_e32 v0, v4\n\t" \
    "v_permlane32_swap_b32_e32 v1, v5\n\t" \
    "v_permlane32_swap_b32_e32 v2, v6\n\t" \
    "v_permlane32_swap_b32_e32 v3, v7\n\t" \
    "v_add_f32_e32 v0, v0, v4\n\t" \
    "v_add_f32_e32 v1, v1, v5\n\t" \
    "v_add_f32_e32 v2, v2, v6\n\t" \
    "v_add_f32_e32 v3, v3, v7\n\t" \
    "v_cvt_pk_bf16_f32 v212, v0, v1\n\t" \
    "v_cvt_pk_bf16_f32 v213, v2, v3\n\t" \
    "v_min_u32_e32 v212, -2, v212\n\t" \
    "v_min_u32_e32 v213, -2, v213\n\t" \
    "buffer_store_dwordx2 v[212:213], v241, s[40:43], s56 offen sc1\n\t" \
    "v_mov_b64_e32 v[32:33], 0\n\t" \
    "v_mov_b64_e32 v[34:35], 0\n\t" \
    "v_mov_b64_e32 v[36:37], 0\n\t" \
    "v_mov_b64_e32 v[38:39], 0\n\t" \
    "v_mov_b64_e32 v[40:41], 0\n\t" \
    "v_mov_b64_e32 v[42:43], 0\n\t" \
    "v_mov_b64_e32 v[44:45], 0\n\t" \
    "v_mov_b64_e32 v[46:47], 0\n\t" \
    "v_mov_b64_e32 v[48:49], 0\n\t" \
    "v_mov_b64_e32 v[50:51], 0\n\t" \
    "v_mov_b64_e32 v[52:53], 0\n\t" \
    "v_mov_b64_e32 v[54:55], 0\n\t" \
    "ds_read_b128 v[180:183], v233 offset:0\n\t" \
    "ds_read_b128 v[184:187], v233 offset:1024\n\t" \
    "ds_read_b128 v[188:191], v233 offset:8192\n\t" \
    "ds_read_b128 v[192:195], v233 offset:9216\n\t" \
    "ds_read_b128 v[196:199], v233 offset:16384\n\t" \
    "ds_read_b128 v[200:203], v233 offset:17408\n\t" \
    "ds_read_b128 v[204:207], v233 offset:24576\n\t" \
    "ds_read_b128 v[208:211], v233 offset:25600\n\t" \
    "s_cmp_lg_u32 s65, 3\n\t" \
    "s_cbranch_scc1 LRH_nored_%=\n\t" \
    "s_mov_b32 s66, s45\n\t" \
    "s_cmp_eq_u32 s66, 0\n\t" \
    "s_cbranch_scc1 LRH_d1_e_%=\n\t" \
    "LRH_d1_%=:\n\t" \
    "s_sub_u32 s66, s66, 1\n\t" \
    "s_cmp_lg_u32 s66, 0\n\t" \
    "s_cbranch_scc1 LRH_d1_%=\n\t" \
    "LRH_d1_e_%=:\n\t" \
    "s_mov_b32 s63, 400000\n\t" \
    "LRH_p1_%=:\n\t" \
    "buffer_load_dwordx4 v[16:19], v242, s[40:43], s56 offen offset:0 sc1\n\t" \
    "buffer_load_dwordx4 v[20:23], v242, s[40:43], s69 offen offset:0 sc1\n\t" \
    "s_waitcnt vmcnt(0)\n\t" \
    "v_max3_u32 v251, v16, v17, v18\n\t" \
    "v_max_u32_e32 v251, v251, v19\n\t" \
    "v_max3_u32 v251, v20, v21, v251\n\t" \
    "v_max3_u32 v251, v22, v23, v251\n\t" \
    "v_cmp_eq_u32_e32 vcc, -1, v251\n\t" \
    "s_cmp_eq_u64 vcc, 0\n\t" \
    "s_cbranch_scc1 LRH_p1_ok_%=\n\t" \
    "s_cmp_lg_u32 s64, 0\n\t" \
    "s_cbranch_scc1 LRH_p1_ok_%=\n\t" \
    "s_sub_u32 s63, s63, 1\n\t" \
    "s_cmp_lg_u32 s63, 0\n\t" \
    "s_cbranch_scc1 LRH_p1_%=\n\t" \
    "s_mov_b32 s64, 1\n\t" \
    "LRH_p1_ok_%=:\n\t" \
    "v_mfma_f32_16x16x32_bf16 v[24:27], v[174:177], v[16:19], 0\n\t" \
    "v_mfma_f32_16x16x32_bf16 v[24:27], v[174:177], v[20:23], v[24:27]\n\t" \
    "s_nop 7\n\t" \
    "v_add_f32_dpp v24, v24, v24 row_ror:4 row_mask:0xf bank_mask:0xf\n\t" \
    "v_add_f32_dpp v25, v25, v25 row_ror:4 row_mask:0xf bank_mask:0xf\n\t" \
    "v_add_f32_dpp v26, v26, v26 row_ror:4 row_mask:0xf bank_mask:0xf\n\t" \
    "v_add_f32_dpp v27, v27, v27 row_ror:4 row_mask:0xf bank_mask:0xf\n\t" \
    "v_add_f32_dpp v24, v24, v24 row_ror:8 row_mask:0xf bank_mask:0xf\n\t" \
    "v_add_f32_dpp v25, v25, v25 row_ror:8 row_mask:0xf bank_mask:0xf\n\t" \
    "v_add_f32_dpp v26, v26, v26 row_ror:8 row_mask:0xf bank_mask:0xf\n\t" \
    "v_add_f32_dpp v27, v27, v27 row_ror:8 row_mask:0xf bank_mask:0xf\n\t" \
    "v_cvt_pk_bf16_f32 v212, v24, v25\n\t" \
    "v_cvt_pk_bf16_f32 v213, v26, v27\n\t" \
    "v_min_u32_e32 v212, -2, v212\n\t" \
    "v_min_u32_e32 v213, -2, v213\n\t" \
    "buffer_store_dwordx2 v[212:213], v244, s[40:43], s58 offen sc1\n\t" \
    "buffer_store_dwordx2 v[178:179], v244, s[40:43], s59 offen sc1\n\t" \
    "LRH_nored_%=:\n\t" \
    "s_barrier\n\t" \
    "s_mov_b32 s75, s58\n\t" \
    "s_add_u32 s44, s44, 1\n\t" \
    "s_and_b32 s66, s44, 1\n\t" \
    "s_lshl_b32 s56, s66, 19\n\t" \
    "s_xor_b32 s57, s56, 524288\n\t" \
    "s_add_u32 s69, s56, 0x10000\n\t" \
    "s_add_u32 s66, s44, 1\n\t" \
    "s_and_b32 s67, s66, 3\n\t" \
    "s_lshl_b32 s58, s67, 14\n\t" \
    "s_add_u32 s67, s44, 3\n\t" \
    "s_and_b32 s67, s67, 3\n\t" \
    "s_lshl_b32 s59, s67, 14\n\t" \
    "s_lshl_b32 s66, s66, 18\n\t" \
    "s_add_u32 s60, s66, 25165824\n\t" \
    "s_add_u32 s61, s66, 41943040\n\t" \
    "s_lshl_b32 s62, s44, 17\n\t" \
    "s_mov_b32 s66, s46\n\t" \
    "s_cmp_eq_u32 s66, 0\n\t" \
    "s_cbranch_scc1 LRH_d2_e_%=\n\t" \
    "LRH_d2_%=:\n\t" \
    "s_sub_u32 s66, s66, 1\n\t" \
    "s_cmp_lg_u32 s66, 0\n\t" \
    "s_cbranch_scc1 LRH_d2_%=\n\t" \
    "LRH_d2_e_%=:\n\t" \
    "s_mov_b32 s63, 400000\n\t" \
    "LRH_p2_%=:\n\t" \
    "buffer_load_dwordx4 v[16:19], v245, s[40:43], s75 offen offset:0 sc1\n\t" \
    "buffer_load_dwordx4 v[20:23], v245, s[40:43], s75 offen offset:1024 sc1\n\t" \
    "s_waitcnt vmcnt(0)\n\t" \
    "v_max3_u32 v251, v16, v17, v18\n\t" \
    "v_max_u32_e32 v251, v251, v19\n\t" \
    "v_max3_u32 v251, v20, v21, v251\n\t" \
    "v_max3_u32 v251, v22, v23, v251\n\t" \
    "v_cmp_eq_u32_e32 vcc, -1, v251\n\t" \
    "s_cmp_eq_u64 vcc, 0\n\t" \
    "s_cbranch_scc1 LRH_p2_ok_%=\n\t" \
    "s_cmp_lg_u32 s64, 0\n\t" \
    "s_cbranch_scc1 LRH_p2_ok_%=\n\t" \
    "s_sub_u32 s63, s63, 1\n\t" \
    "s_cmp_lg_u32 s63, 0\n\t" \
    "s_cbranch_scc1 LRH_p2_%=\n\t" \
    "s_mov_b32 s64, 1\n\t" \
    "LRH_p2_ok_%=:\n\t" \
    "ds_write_b128 v240, v[16:19]\n\t" \
    "ds_write_b128 v240, v[20:23] offset:1536\n\t" \
    "s_waitcnt lgkmcnt(0)\n\t" \
    "s_barrier\n\t" \
    "s_cmp_lt_u32 s44, 63\n\t" \
    "s_cbranch_scc1 LRH_loop_%=\n\t" \
    "s_branch LRH_tail_%=\n\t" \
    "LRH_h1poll_%=:\n\t" \
    "s_mov_b32 s63, 400000\n\t" \
    "LRH_h1p_%=:\n\t" \
    "buffer_load_dword v243, off, s[40:43], s48 sc1\n\t" \
    "s_waitcnt vmcnt(0)\n\t" \
    "v_readfirstlane_b32 s47, v243\n\t" \
    "s_add_u32 s66, s44, 2\n\t" \
    "s_cmp_ge_u32 s47, s66\n\t" \
    "s_cbranch_scc1 LRH_h1back_%=\n\t" \
    "s_cmp_lg_u32 s64, 0\n\t" \
    "s_cbranch_scc1 LRH_h1back_%=\n\t" \
    "s_sleep 2\n\t" \
    "s_sub_u32 s63, s63, 1\n\t" \
    "s_cmp_lg_u32 s63, 0\n\t" \
    "s_cbranch_scc1 LRH_h1p_%=\n\t" \
    "s_mov_b32 s64, 1\n\t" \
    "s_branch LRH_h1back_%=\n\t" \
    "LRH_slow_%=:\n\t" \
    "s_nop 11\n\t" \
    "v_max3_f32 v251, v224, v225, v226\n\t" \
    "v_max3_f32 v252, v227, v228, v229\n\t" \
    "v_max3_f32 v251, v251, v230, v231\n\t" \
    "v_max_f32_e32 v251, v251, v252\n\t" \
    "s_nop 1\n\t" \
    "v_max_f32_dpp v251, v251, v251 row_ror:8 row_mask:0xf bank_mask:0xf\n\t" \
    "v_mov_b32_e32 v252, v251\n\t" \
    "s_nop 1\n\t" \
    "v_permlane16_swap_b32_e32 v251, v252\n\t" \
    "v_max_f32_e32 v251, v251, v252\n\t" \
    "v_mov_b32_e32 v252, v251\n\t" \
    "s_nop 1\n\t" \
    "v_permlane32_swap_b32_e32 v251, v252\n\t" \
    "v_max_f32_e32 v251, v251, v252\n\t" \
    "v_sub_f32_e32 v224, v224, v251\n\t" \
    "v_sub_f32_e32 v228, v228, v251\n\t" \
    "v_sub_f32_e32 v225, v225, v251\n\t" \
    "v_sub_f32_e32 v229, v229, v251\n\t" \
    "v_sub_f32_e32 v226, v226, v251\n\t" \
    "v_sub_f32_e32 v230, v230, v251\n\t" \
    "v_sub_f32_e32 v227, v227, v251\n\t" \
    "v_sub_f32_e32 v231, v231, v251\n\t" \
    "v_exp_f32_e32 v220, v224\n\t" \
    "v_exp_f32_e32 v221, v225\n\t" \
    "v_exp_f32_e32 v222, v226\n\t" \
    "v_exp_f32_e32 v223, v227\n\t" \
    "v_cvt_pk_bf16_f32 v212, v220, v221\n\t" \
    "v_cvt_pk_bf16_f32 v213, v222, v223\n\t" \
    "s_nop 1\n\t" \
    "v_mfma_f32_16x16x16_bf16 v[32:35], v[160:161], v[212:213], 0\n\t" \
    "v_exp_f32_e32 v216, v228\n\t" \
    "v_mfma_f32_16x16x16_bf16 v[44:47], v[166:167], v[212:213], 0\n\t" \
    "v_exp_f32_e32 v217, v229\n\t" \
    "v_mfma_f32_16x16x16_bf16 v[40:43], v[164:165], v[212:213], 0\n\t" \
    "v_exp_f32_e32 v218, v230\n\t" \
    "v_mfma_f32_16x16x16_bf16 v[36:39], v[162:163], v[212:213], 0\n\t" \
    "v_exp_f32_e32 v219, v231\n\t" \
    "v_cvt_pk_bf16_f32 v214, v216, v217\n\t" \
    "v_cvt_pk_bf16_f32 v215, v218, v219\n\t" \
    "v_add_f32_e32 v251, v220, v221\n\t" \
    "v_add_f32_e32 v252, v222, v223\n\t" \
    "v_add_f32_e32 v253, v216, v217\n\t" \
    "v_add_f32_e32 v254, v218, v219\n\t" \
    "v_add_f32_e32 v251, v251, v252\n\t" \
    "v_add_f32_e32 v253, v253, v254\n\t" \
    "v_cndmask_b32_e64 v253, 0, v253, s[54:55]\n\t" \
    "v_mfma_f32_16x16x16_bf16 v[32:35], v[8:9], v[214:215], v[32:35]\n\t" \
    "v_mfma_f32_16x16x16_bf16 v[40:43], v[10:11], v[214:215], v[40:43]\n\t" \
    "v_add_f32_e32 v251, v251, v253\n\t" \
    "s_nop 1\n\t" \
    "v_add_f32_dpp v251, v251, v251 row_ror:8 row_mask:0xf bank_mask:0xf\n\t" \
    "v_mov_b32_e32 v254, v251\n\t" \
    "s_nop 1\n\t" \
    "v_permlane16_swap_b32_e32 v251, v254\n\t" \
    "v_add_f32_e32 v251, v251, v254\n\t" \
    "v_mov_b32_e32 v254, v251\n\t" \
    "s_nop 1\n\t" \
    "v_permlane32_swap_b32_e32 v251, v254\n\t" \
    "v_add_f32_e32 v251, v251, v254\n\t" \
    "v_rcp_f32_e32 v251, v251\n\t" \
    "s_nop 9\n\t" \
    "s_branch LRH_join_%=\n\t" \
    "LRH_tail_%=:\n\t" \
    "v_mov_b32_e32 v251, 63\n\t" \
    "buffer_store_dword v251, v249, s[40:43], 0 offen sc1\n\t" \
    "ds_read_b64_tr_b16 v[0:1], v232 offset:0\n\t" \
    "ds_read_b64_tr_b16 v[2:3], v232 offset:128\n\t" \
    "ds_read_b64_tr_b16 v[4:5], v232 offset:1536\n\t" \
    "ds_read_b64_tr_b16 v[6:7], v232 offset:1664\n\t" \
    "ds_read_b64_tr_b16 v[8:9], v232 offset:3072\n\t" \
    "ds_read_b64_tr_b16 v[10:11], v232 offset:3200\n\t" \
    "ds_read_b64_tr_b16 v[12:13], v232 offset:4608\n\t" \
    "ds_read_b64_tr_b16 v[14:15], v232 offset:4736\n\t" \
    "ds_read_b64_tr_b16 v[16:17], v232 offset:6144\n\t" \
    "ds_read_b64_tr_b16 v[18:19], v232 offset:6272\n\t" \
    "ds_read_b64_tr_b16 v[20:21], v232 offset:7680\n\t" \
    "ds_read_b64_tr_b16 v[22:23], v232 offset:7808\n\t" \
    "ds_read_b64_tr_b16 v[24:25], v232 offset:9216\n\t" \
    "ds_read_b64_tr_b16 v[26:27], v232 offset:9344\n\t" \
    "ds_read_b64_tr_b16 v[28:29], v232 offset:10752\n\t" \
    "ds_read_b64_tr_b16 v[30:31], v232 offset:10880\n\t" \
    "s_cmp_eq_u64 s[76:77], 0\n\t" \
    "s_cbranch_scc1 LRH_tailv_%=\n\t" \
    "s_waitcnt lgkmcnt(14)\n\t" \
    "v_smfmac_f32_16x16x64_bf16 v[40:43], v[0:3], a[128:135], v250\n\t" \
    "v_smfmac_f32_16x16x64_bf16 v[44:47], v[0:3], a[192:199], v250\n\t" \
    "s_waitcnt lgkmcnt(12)\n\t" \
    "v_smfmac_f32_16x16x64_bf16 v[40:43], v[4:7], a[136:143], v250\n\t" \
    "v_smfmac_f32_16x16x64_bf16 v[44:47], v[4:7], a[200:207], v250\n\t" \
    "s_waitcnt lgkmcnt(10)\n\t" \
    "v_smfmac_f32_16x16x64_bf16 v[40:43], v[8:11], a[144:151], v250\n\t" \
    "v_smfmac_f32_16x16x64_bf16 v[44:47], v[8:11], a[208:215], v250\n\t" \
    "s_waitcnt lgkmcnt(8)\n\t" \
    "v_smfmac_f32_16x16x64_bf16 v[40:43], v[12:15], a[152:159], v250\n\t" \
    "v_smfmac_f32_16x16x64_bf16 v[44:47], v[12:15], a[216:223], v250\n\t" \
    "s_waitcnt lgkmcnt(6)\n\t" \
    "v_smfmac_f32_16x16x64_bf16 v[40:43], v[16:19], a[160:167], v250\n\t" \
    "v_smfmac_f32_16x16x64_bf16 v[44:47], v[16:19], a[224:231], v250\n\t" \
    "s_waitcnt lgkmcnt(4)\n\t" \
    "v_smfmac_f32_16x16x64_bf16 v[40:43], v[20:23], a[168:175], v250\n\t" \
    "v_smfmac_f32_16x16x64_bf16 v[44:47], v[20:23], a[232:239], v250\n\t" \
    "s_waitcnt lgkmcnt(2)\n\t" \
    "v_smfmac_f32_16x16x64_bf16 v[40:43], v[24:27], a[176:183], v250\n\t" \
    "v_smfmac_f32_16x16x64_bf16 v[44:47], v[24:27], a[240:247], v250\n\t" \
    "s_waitcnt lgkmcnt(0)\n\t" \
    "v_smfmac_f32_16x16x64_bf16 v[40:43], v[28:31], a[184:191], v250\n\t" \
    "v_smfmac_f32_16x16x64_bf16 v[44:47], v[28:31], a[248:255], v250\n\t" \
    "s_nop 4\n\t" \
    "v_cvt_pk_bf16_f32 v212, v40, v41\n\t" \
    "v_cvt_pk_bf16_f32 v213, v42, v43\n\t" \
    "s_nop 0\n\t" \
    "v_cvt_pk_bf16_f32 v214, v44, v45\n\t" \
    "v_cvt_pk_bf16_f32 v215, v46, v47\n\t" \
    "s_nop 1\n\t" \
    "v_mfma_f32_16x16x32_bf16 v[40:43], v[212:215], v[170:173], 0\n\t" \
    "v_mfma_f32_16x16x32_bf16 v[44:47], v[212:215], v[168:171], 0\n\t" \
    "s_nop 5\n\t" \
    "v_cvt_pk_bf16_f32 v220, v40, v41\n\t" \
    "v_cvt_pk_bf16_f32 v221, v42, v43\n\t" \
    "v_cvt_pk_bf16_f32 v222, v44, v45\n\t" \
    "v_cvt_pk_bf16_f32 v223, v46, v47\n\t" \
    "s_nop 1\n\t" \
    "buffer_store_dwordx4 v[220:223], v247, s[40:43], s62 offen sc1\n\t" \
    "s_branch LRH_tailst_%=\n\t" \
    "LRH_tailv_%=:\n\t" \
    "s_waitcnt lgkmcnt(0)\n\t" \
    "s_nop 3\n\t" \
    "v_smfmac_f32_16x16x64_bf16 v[48:51], v[0:3], v[180:187], v250\n\t" \
    "v_smfmac_f32_16x16x64_bf16 v[52:55], v[0:3], v[188:195], v250\n\t" \
    "v_smfmac_f32_16x16x64_bf16 v[48:51], v[4:7], v[196:203], v250\n\t" \
    "v_smfmac_f32_16x16x64_bf16 v[52:55], v[4:7], v[204:211], v250\n\t" \
    "v_smfmac_f32_16x16x64_bf16 v[48:51], v[8:11], v[56:63], v250\n\t" \
    "v_smfmac_f32_16x16x64_bf16 v[52:55], v[8:11], v[64:71], v250\n\t" \
    "v_smfmac_f32_16x16x64_bf16 v[48:51], v[12:15], v[72:79], v250\n\t" \
    "v_smfmac_f32_16x16x64_bf16 v[52:55], v[12:15], v[80:87], v250\n\t" \
    "v_smfmac_f32_16x16x64_bf16 v[48:51], v[16:19], v[88:95], v250\n\t" \
    "v_smfmac_f32_16x16x64_bf16 v[52:55], v[16:19], v[96:103], v250\n\t" \
    "v_smfmac_f32_16x16x64_bf16 v[48:51], v[20:23], v[104:111], v250\n\t" \
    "v_smfmac_f32_16x16x64_bf16 v[52:55], v[20:23], v[112:119], v250\n\t" \
    "v_smfmac_f32_16x16x64_bf16 v[48:51], v[24:27], v[120:127], v250\n\t" \
    "v_smfmac_f32_16x16x64_bf16 v[52:55], v[24:27], v[128:135], v250\n\t" \
    "v_smfmac_f32_16x16x64_bf16 v[48:51], v[28:31], v[136:143], v250\n\t" \
    "v_smfmac_f32_16x16x64_bf16 v[52:55], v[28:31], v[144:151], v250\n\t" \
    "s_nop 4\n\t" \
    "v_cvt_pk_bf16_f32 v220, v48, v49\n\t" \
    "v_cvt_pk_bf16_f32 v221, v50, v51\n\t" \
    "s_nop 0\n\t" \
    "v_cvt_pk_bf16_f32 v222, v52, v53\n\t" \
    "v_cvt_pk_bf16_f32 v223, v54, v55\n\t" \
    "s_nop 1\n\t" \
    "buffer_store_dwordx4 v[220:223], v248, s[40:43], s62 offen sc1\n\t" \
    "LRH_tailst_%=:\n\t" \
    "s_waitcnt vmcnt(0)\n\t" \
    "s_waitcnt lgkmcnt(0)\n\t" \
    "s_barrier\n\t" \
    "v_mov_b32_e32 v251, 64\n\t" \
    "buffer_store_dword v251, v249, s[40:43], 0 offen sc1\n\t" \
    "s_waitcnt vmcnt(0)\n\t" \
    ""
#define RH_LOADW_Q0 \
    "ds_read_b64_tr_b16 a[0:1], %0 offset:0\n\t" \
    "ds_read_b64_tr_b16 a[2:3], %0 offset:1024\n\t" \
    "ds_read_b64_tr_b16 a[4:5], %0 offset:8192\n\t" \
    "ds_read_b64_tr_b16 a[6:7], %0 offset:9216\n\t" \
    "ds_read_b64_tr_b16 a[8:9], %0 offset:16384\n\t" \
    "ds_read_b64_tr_b16 a[10:11], %0 offset:17408\n\t" \
    "ds_read_b64_tr_b16 a[12:13], %0 offset:24576\n\t" \
    "ds_read_b64_tr_b16 a[14:15], %0 offset:25600\n\t" \
    "s_waitcnt lgkmcnt(0)\n\t" \
    "ds_read_b64_tr_b16 a[64:65], %1 offset:0\n\t" \
    "ds_read_b64_tr_b16 a[66:67], %1 offset:1024\n\t" \
    "ds_read_b64_tr_b16 a[68:69], %1 offset:8192\n\t" \
    "ds_read_b64_tr_b16 a[70:71], %1 offset:9216\n\t" \
    "ds_read_b64_tr_b16 a[72:73], %1 offset:16384\n\t" \
    "ds_read_b64_tr_b16 a[74:75], %1 offset:17408\n\t" \
    "ds_read_b64_tr_b16 a[76:77], %1 offset:24576\n\t" \
    "ds_read_b64_tr_b16 a[78:79], %1 offset:25600\n\t" \
    "s_waitcnt lgkmcnt(0)\n\t" \
    ""
#define RH_CLOB_Q0 "a0", "a1", "a2", "a3", "a4", "a5", "a6", "a7", "a8", "a9", "a10", "a11", "a12", "a13", "a14", "a15", "a64", "a65", "a66", "a67", "a68", "a69", "a70", "a71", "a72", "a73", "a74", "a75", "a76", "a77", "a78", "a79"
#define RH_LOADW_Q1 \
    "ds_read_b64_tr_b16 a[16:17], %0 offset:0\n\t" \
    "ds_read_b64_tr_b16 a[18:19], %0 offset:1024\n\t" \
    "ds_read_b64_tr_b16 a[20:21], %0 offset:8192\n\t" \
    "ds_read_b64_tr_b16 a[22:23], %0 offset:9216\n\t" \
    "ds_read_b64_tr_b16 a[24:25], %0 offset:16384\n\t" \
    "ds_read_b64_tr_b16 a[26:27], %0 offset:17408\n\t" \
    "ds_read_b64_tr_b16 a[28:29], %0 offset:24576\n\t" \
    "ds_read_b64_tr_b16 a[30:31], %0 offset:25600\n\t" \
    "s_waitcnt lgkmcnt(0)\n\t" \
    "ds_read_b64_tr_b16 a[80:81], %1 offset:0\n\t" \
    "ds_read_b64_tr_b16 a[82:83], %1 offset:1024\n\t" \
    "ds_read_b64_tr_b16 a[84:85], %1 offset:8192\n\t" \
    "ds_read_b64_tr_b16 a[86:87], %1 offset:9216\n\t" \
    "ds_read_b64_tr_b16 a[88:89], %1 offset:16384\n\t" \
    "ds_read_b64_tr_b16 a[90:91], %1 offset:17408\n\t" \
    "ds_read_b64_tr_b16 a[92:93], %1 offset:24576\n\t" \
    "ds_read_b64_tr_b16 a[94:95], %1 offset:25600\n\t" \
    "s_waitcnt lgkmcnt(0)\n\t" \
    ""
#define RH_CLOB_Q1 "a16", "a17", "a18", "a19", "a20", "a21", "a22", "a23", "a24", "a25", "a26", "a27", "a28", "a29", "a30", "a31", "a80", "a81", "a82", "a83", "a84", "a85", "a86", "a87", "a88", "a89", "a90", "a91", "a92", "a93", "a94", "a95"
#define RH_LOADW_Q2 \
    "ds_read_b64_tr_b16 a[32:33], %0 offset:0\n\t" \
    "ds_read_b64_tr_b16 a[34:35], %0 offset:1024\n\t" \
    "ds_read_b64_tr_b16 a[36:37], %0 offset:8192\n\t" \
    "ds_read_b64_tr_b16 a[38:39], %0 offset:9216\n\t" \
    "ds_read_b64_tr_b16 a[40:41], %0 offset:16384\n\t" \
    "ds_read_b64_tr_b16 a[42:43], %0 offset:17408\n\t" \
    "ds_read_b64_tr_b16 a[44:45], %0 offset:24576\n\t" \
    "ds_read_b64_tr_b16 a[46:47], %0 offset:25600\n\t" \
    "s_waitcnt lgkmcnt(0)\n\t" \
    "ds_read_b64_tr_b16 a[96:97], %1 offset:0\n\t" \
    "ds_read_b64_tr_b16 a[98:99], %1 offset:1024\n\t" \
    "ds_read_b64_tr_b16 a[100:101], %1 offset:8192\n\t" \
    "ds_read_b64_tr_b16 a[102:103], %1 offset:9216\n\t" \
    "ds_read_b64_tr_b16 a[104:105], %1 offset:16384\n\t" \
    "ds_read_b64_tr_b16 a[106:107], %1 offset:17408\n\t" \
    "ds_read_b64_tr_b16 a[108:109], %1 offset:24576\n\t" \
    "ds_read_b64_tr_b16 a[110:111], %1 offset:25600\n\t" \
    "s_waitcnt lgkmcnt(0)\n\t" \
    ""
#define RH_CLOB_Q2 "a32", "a33", "a34", "a35", "a36", "a37", "a38", "a39", "a40", "a41", "a42", "a43", "a44", "a45", "a46", "a47", "a96", "a97", "a98", "a99", "a100", "a101", "a102", "a103", "a104", "a105", "a106", "a107", "a108", "a109", "a110", "a111"
#define RH_LOADW_Q3 \
    "ds_read_b64_tr_b16 a[48:49], %0 offset:0\n\t" \
    "ds_read_b64_tr_b16 a[50:51], %0 offset:1024\n\t" \
    "ds_read_b64_tr_b16 a[52:53], %0 offset:8192\n\t" \
    "ds_read_b64_tr_b16 a[54:55], %0 offset:9216\n\t" \
    "ds_read_b64_tr_b16 a[56:57], %0 offset:16384\n\t" \
    "ds_read_b64_tr_b16 a[58:59], %0 offset:17408\n\t" \
    "ds_read_b64_tr_b16 a[60:61], %0 offset:24576\n\t" \
    "ds_read_b64_tr_b16 a[62:63], %0 offset:25600\n\t" \
    "s_waitcnt lgkmcnt(0)\n\t" \
    "ds_read_b64_tr_b16 a[112:113], %1 offset:0\n\t" \
    "ds_read_b64_tr_b16 a[114:115], %1 offset:1024\n\t" \
    "ds_read_b64_tr_b16 a[116:117], %1 offset:8192\n\t" \
    "ds_read_b64_tr_b16 a[118:119], %1 offset:9216\n\t" \
    "ds_read_b64_tr_b16 a[120:121], %1 offset:16384\n\t" \
    "ds_read_b64_tr_b16 a[122:123], %1 offset:17408\n\t" \
    "ds_read_b64_tr_b16 a[124:125], %1 offset:24576\n\t" \
    "ds_read_b64_tr_b16 a[126:127], %1 offset:25600\n\t" \
    "s_waitcnt lgkmcnt(0)\n\t" \
    ""
#define RH_CLOB_Q3 "a48", "a49", "a50", "a51", "a52", "a53", "a54", "a55", "a56", "a57", "a58", "a59", "a60", "a61", "a62", "a63", "a112", "a113", "a114", "a115", "a116", "a117", "a118", "a119", "a120", "a121", "a122", "a123", "a124", "a125", "a126", "a127"
#define RH_LOADW_K0 \
    "ds_read_b64_tr_b16 a[128:129], %0 offset:0\n\t" \
    "ds_read_b64_tr_b16 a[130:131], %0 offset:1024\n\t" \
    "ds_read_b64_tr_b16 a[132:133], %0 offset:8192\n\t" \
    "ds_read_b64_tr_b16 a[134:135], %0 offset:9216\n\t" \
    "ds_read_b64_tr_b16 a[136:137], %0 offset:16384\n\t" \
    "ds_read_b64_tr_b16 a[138:139], %0 offset:17408\n\t" \
    "ds_read_b64_tr_b16 a[140:141], %0 offset:24576\n\t" \
    "ds_read_b64_tr_b16 a[142:143], %0 offset:25600\n\t" \
    "s_waitcnt lgkmcnt(0)\n\t" \
    "ds_read_b64_tr_b16 a[192:193], %1 offset:0\n\t" \
    "ds_read_b64_tr_b16 a[194:195], %1 offset:1024\n\t" \
    "ds_read_b64_tr_b16 a[196:197], %1 offset:8192\n\t" \
    "ds_read_b64_tr_b16 a[198:199], %1 offset:9216\n\t" \
    "ds_read_b64_tr_b16 a[200:201], %1 offset:16384\n\t" \
    "ds_read_b64_tr_b16 a[202:203], %1 offset:17408\n\t" \
    "ds_read_b64_tr_b16 a[204:205], %1 offset:24576\n\t" \
    "ds_read_b64_tr_b16 a[206:207], %1 offset:25600\n\t" \
    "s_waitcnt lgkmcnt(0)\n\t" \
    ""
#define RH_CLOB_K0 "a128", "a129", "a130", "a131", "a132", "a133", "a134", "a135", "a136", "a137", "a138", "a139", "a140", "a141", "a142", "a143", "a192", "a193", "a194", "a195", "a196", "a197", "a198", "a199", "a200", "a201", "a202", "a203", "a204", "a205", "a206", "a207"
#define RH_LOADW_K1 \
    "ds_read_b64_tr_b16 a[144:145], %0 offset:0\n\t" \
    "ds_read_b64_tr_b16 a[146:147], %0 offset:1024\n\t" \
    "ds_read_b64_tr_b16 a[148:149], %0 offset:8192\n\t" \
    "ds_read_b64_tr_b16 a[150:151], %0 offset:9216\n\t" \
    "ds_read_b64_tr_b16 a[152:153], %0 offset:16384\n\t" \
    "ds_read_b64_tr_b16 a[154:155], %0 offset:17408\n\t" \
    "ds_read_b64_tr_b16 a[156:157], %0 offset:24576\n\t" \
    "ds_read_b64_tr_b16 a[158:159], %0 offset:25600\n\t" \
    "s_waitcnt lgkmcnt(0)\n\t" \
    "ds_read_b64_tr_b16 a[208:209], %1 offset:0\n\t" \
    "ds_read_b64_tr_b16 a[210:211], %1 offset:1024\n\t" \
    "ds_read_b64_tr_b16 a[212:213], %1 offset:8192\n\t" \
    "ds_read_b64_tr_b16 a[214:215], %1 offset:9216\n\t" \
    "ds_read_b64_tr_b16 a[216:217], %1 offset:16384\n\t" \
    "ds_read_b64_tr_b16 a[218:219], %1 offset:17408\n\t" \
    "ds_read_b64_tr_b16 a[220:221], %1 offset:24576\n\t" \
    "ds_read_b64_tr_b16 a[222:223], %1 offset:25600\n\t" \
    "s_waitcnt lgkmcnt(0)\n\t" \
    ""
#define RH_CLOB_K1 "a144", "a145", "a146", "a147", "a148", "a149", "a150", "a151", "a152", "a153", "a154", "a155", "a156", "a157", "a158", "a159", "a208", "a209", "a210", "a211", "a212", "a213", "a214", "a215", "a216", "a217", "a218", "a219", "a220", "a221", "a222", "a223"
#define RH_LOADW_K2 \
    "ds_read_b64_tr_b16 a[160:161], %0 offset:0\n\t" \
    "ds_read_b64_tr_b16 a[162:163], %0 offset:1024\n\t" \
    "ds_read_b64_tr_b16 a[164:165], %0 offset:8192\n\t" \
    "ds_read_b64_tr_b16 a[166:167], %0 offset:9216\n\t" \
    "ds_read_b64_tr_b16 a[168:169], %0 offset:16384\n\t" \
    "ds_read_b64_tr_b16 a[170:171], %0 offset:17408\n\t" \
    "ds_read_b64_tr_b16 a[172:173], %0 offset:24576\n\t" \
    "ds_read_b64_tr_b16 a[174:175], %0 offset:25600\n\t" \
    "s_waitcnt lgkmcnt(0)\n\t" \
    "ds_read_b64_tr_b16 a[224:225], %1 offset:0\n\t" \
    "ds_read_b64_tr_b16 a[226:227], %1 offset:1024\n\t" \
    "ds_read_b64_tr_b16 a[228:229], %1 offset:8192\n\t" \
    "ds_read_b64_tr_b16 a[230:231], %1 offset:9216\n\t" \
    "ds_read_b64_tr_b16 a[232:233], %1 offset:16384\n\t" \
    "ds_read_b64_tr_b16 a[234:235], %1 offset:17408\n\t" \
    "ds_read_b64_tr_b16 a[236:237], %1 offset:24576\n\t" \
    "ds_read_b64_tr_b16 a[238:239], %1 offset:25600\n\t" \
    "s_waitcnt lgkmcnt(0)\n\t" \
    ""
#define RH_CLOB_K2 "a160", "a161", "a162", "a163", "a164", "a165", "a166", "a167", "a168", "a169", "a170", "a171", "a172", "a173", "a174", "a175", "a224", "a225", "a226", "a227", "a228", "a229", "a230", "a231", "a232", "a233", "a234", "a235", "a236", "a237", "a238", "a239"
#define RH_LOADW_K3 \
    "ds_read_b64_tr_b16 a[176:177], %0 offset:0\n\t" \
    "ds_read_b64_tr_b16 a[178:179], %0 offset:1024\n\t" \
    "ds_read_b64_tr_b16 a[180:181], %0 offset:8192\n\t" \
    "ds_read_b64_tr_b16 a[182:183], %0 offset:9216\n\t" \
    "ds_read_b64_tr_b16 a[184:185], %0 offset:16384\n\t" \
    "ds_read_b64_tr_b16 a[186:187], %0 offset:17408\n\t" \
    "ds_read_b64_tr_b16 a[188:189], %0 offset:24576\n\t" \
    "ds_read_b64_tr_b16 a[190:191], %0 offset:25600\n\t" \
    "s_waitcnt lgkmcnt(0)\n\t" \
    "ds_read_b64_tr_b16 a[240:241], %1 offset:0\n\t" \
    "ds_read_b64_tr_b16 a[242:243], %1 offset:1024\n\t" \
    "ds_read_b64_tr_b16 a[244:245], %1 offset:8192\n\t" \
    "ds_read_b64_tr_b16 a[246:247], %1 offset:9216\n\t" \
    "ds_read_b64_tr_b16 a[248:249], %1 offset:16384\n\t" \
    "ds_read_b64_tr_b16 a[250:251], %1 offset:17408\n\t" \
    "ds_read_b64_tr_b16 a[252:253], %1 offset:24576\n\t" \
    "ds_read_b64_tr_b16 a[254:255], %1 offset:25600\n\t" \
    "s_waitcnt lgkmcnt(0)\n\t" \
    ""
#define RH_CLOB_K3 "a176", "a177", "a178", "a179", "a180", "a181", "a182", "a183", "a184", "a185", "a186", "a187", "a188", "a189", "a190", "a191", "a240", "a241", "a242", "a243", "a244", "a245", "a246", "a247", "a248", "a249", "a250", "a251", "a252", "a253", "a254", "a255"
#define RH_CLOB_V "v0", "v1", "v2", "v3", "v4", "v5", "v6", "v7", "v8", "v9", "v10", "v11", "v12", "v13", "v14", "v15", "v16", "v17", "v18", "v19", "v20", "v21", "v22", "v23", "v24", "v25", "v26", "v27", "v28", "v29", "v30", "v31", "v32", "v33", "v34", "v35", "v36", "v37", "v38", "v39", "v40", "v41", "v42", "v43", "v44", "v45", "v46", "v47", "v48", "v49", "v50", "v51", "v52", "v53", "v54", "v55", "v178", "v179", "v180", "v181", "v182", "v183", "v184", "v185", "v186", "v187", "v188", "v189", "v190", "v191", "v192", "v193", "v194", "v195", "v196", "v197", "v198", "v199", "v200", "v201", "v202", "v203", "v204", "v205", "v206", "v207", "v208", "v209", "v210", "v211", "v212", "v213", "v214", "v215", "v216", "v217", "v218", "v219", "v220", "v221", "v222", "v223", "v224", "v225", "v226", "v227", "v228", "v229", "v230", "v231", "v243", "v251", "v252", "v253", "v254", "v255"
#define RH_CLOB_S "s56", "s57", "s58", "s59", "s60", "s61", "s62", "s63", "s64", "s65", "s66", "s67", "s68", "s69", "s70", "s71"

#define RH_CLOB_S_NOW "s56", "s57", "s58", "s59", "s60", "s61", "s62", "s63", "s64", "s66", "s67", "s68", "s69", "s70", "s71", "s75"

constexpr int NSEG = 64;
constexpr unsigned WS_RING   = 0;
constexpr unsigned RING_P    = 0;
constexpr unsigned RING_M    = 1u << 20;
constexpr unsigned RING_CAL  = (1u << 20) + (1u << 17);
constexpr unsigned WS_FLAGS  = 4u << 20;
constexpr unsigned FL_H1 = 0, FL_KM = 32 * 256, FL_VM = 64 * 256, FL_H2 = 96 * 256, FL_PAR = 160 * 256, FL_ABORT = 161 * 256, FL_ST = 162 * 256;
constexpr unsigned WS_STAT   = (4u << 20) + (1u << 19);
constexpr unsigned WS_XB     = 5u << 20;
constexpr unsigned WS_XF     = 8u << 20;
constexpr unsigned WS_QA     = 12u << 20;
constexpr unsigned WS_KA     = 14u << 20;
constexpr unsigned WS_VA     = 16u << 20;
constexpr unsigned WS_KN     = 18u << 20;
constexpr unsigned WS_OA     = 19u << 20;
constexpr unsigned WS_KF     = 24u << 20;
constexpr unsigned WS_VF     = 40u << 20;
constexpr unsigned WS_KM     = 56u << 20;
constexpr unsigned WS_VM     = 64u << 20;
constexpr unsigned WS_P2     = 72u << 20;
constexpr unsigned WS_DBG    = 136u << 20;
constexpr unsigned WS_END    = 138u << 20;

struct BArgs {
    const char* wq; const char* wk; const char* wv; const char* wo; const char* mem0;
    char* ws;
    char* out;
    int layer;
    int rolemask;
    int w1, w2;
    int dbg;
    int pad_;
};

#define BAR() asm volatile("s_waitcnt lgkmcnt(0)\n\ts_barrier" ::: "memory")
constexpr unsigned SPIN_CAP = 400000u;
DEV int fsw(int k) { return (k & 3) | ((k >> 1) & 4); }
DEV int fo_rh(int e) { return (e & 1) | ((e >> 1) & 2) | ((e >> 1) & 4); }

DEV u32x4 mkrs(const void* p, unsigned bytes) {
    const unsigned long long q = (unsigned long long)p;
    return (u32x4){(unsigned)__builtin_amdgcn_readfirstlane((unsigned)q), (unsigned)__builtin_amdgcn_readfirstlane((unsigned)(q >> 32) & 0xFFFFu), bytes, 0x00020000u};
}
DEV unsigned sgpr(unsigned x) { return __builtin_amdgcn_readfirstlane(x); }
DEV void blds16(u32x4 rs, unsigned voff, unsigned soff, unsigned dst) {
    asm volatile("s_mov_b32 m0, %0\n\ts_nop 0\n\tbuffer_load_dwordx4 %1, %2, %3 offen lds" :: "s"(sgpr(dst)), "v"(voff), "s"(rs), "s"(sgpr(soff)) : "memory");
}
DEV void blds4s(u32x4 rs, unsigned voff, unsigned soff, unsigned dst) {
    asm volatile("s_mov_b32 m0, %0\n\ts_nop 0\n\tbuffer_load_dword %1, %2, %3 offen sc1 lds" :: "s"(sgpr(dst)), "v"(voff), "s"(rs), "s"(sgpr(soff)) : "memory");
}
DEV unsigned ldsoff(ldsp p) { return (unsigned)(size_t)p; }

DEV void stage_w256(const char* src, unsigned stride, ldsp dst, int nrows, int w, int lane) {
    const u32x4 rs = mkrs(src, 0x7FFFFFFFu);
    const int p = lane & 15, f = (lane >> 4) | (((w >> 1) & 1) << 2);
    const unsigned voff = (unsigned)(lane >> 4) * stride + (unsigned)((((p >> 1) ^ f) << 1) | (p & 1)) * 16u;
    const unsigned d0 = sgpr(ldsoff(dst));
    for (int r0 = w * 4; r0 < nrows; r0 += 16) blds16(rs, voff, (unsigned)r0 * stride, d0 + r0 * 256);
}
DEV ldsp w256_addr(ldsp base, int k, int et, int p) { return base + k * 256 + ((et ^ fsw(k)) << 5) + p * 8; }

DEV unsigned poll_flag_ge(rsrc_t ws, unsigned off, unsigned want, bool& dead) {
    unsigned v = 0;
    for (unsigned spin = 0;; ++spin) {
        MEMFENCE();
        v = __builtin_amdgcn_readfirstlane(bld4s(ws, off));
        if (v >= want || dead) break;
        if (spin > SPIN_CAP) { dead = true; break; }
        __builtin_amdgcn_s_sleep(2);
    }
    return v;
}

constexpr int RH_WV = 0, RH_MT = 131072, RH_SX = RH_MT + 12288, RH_OB = RH_SX + 8192, RH_END = RH_OB + 8 * 272;
typedef unsigned u32x6 __attribute__((ext_vector_type(6)));
typedef unsigned u32x3 __attribute__((ext_vector_type(3)));
typedef unsigned u32x8 __attribute__((ext_vector_type(8)));
typedef unsigned u32x16 __attribute__((ext_vector_type(16)));
DEV u32x16 cat16(bf16x16 a, bf16x16 b) { const u32x8 x = __builtin_bit_cast(u32x8, a), y = __builtin_bit_cast(u32x8, b); return __builtin_shufflevector(x, y, 0, 1, 2, 3, 4, 5, 6, 7, 8, 9, 10, 11, 12, 13, 14, 15); }
DEV bf16x16 rh_wfrag(ldsp img, int kbase, int et, int g, int q4, int p4) {
    const int k0 = kbase + 8 * g + q4;
    const bf16x4 r0 = trread(w256_addr(img, k0, et, p4)), r1 = trread(w256_addr(img, k0 + 4, et, p4));
    const bf16x4 r2 = trread(w256_addr(img, k0 + 32, et, p4)), r3 = trread(w256_addr(img, k0 + 36, et, p4));
    return cat8(cat4(r0, r1), cat4(r2, r3));
}

DEV void role_rh(const BArgs& a, ldsp lds, int h, int c) {
    const int tid = threadIdx.x, lane = tid & 63, w = __builtin_amdgcn_readfirstlane(tid >> 6);
    const int n = lane & 15, g = lane >> 4, hs = n >> 3;
    const rsrc_t ws = mkrsrc(a.ws, WS_END);
    const unsigned ring = WS_RING + a.layer * (2u << 20), flags = WS_FLAGS + a.layer * 65536u;
    bool dead = false;
    TLMARK(a.ws, a.layer * 32 + 8);
    if (h == 0 && c == 0 && w == 0) {
        unsigned best[2];
#pragma unroll
        for (int p = 0; p < 2; ++p) { unsigned b = 0xFFFFFFFFu;
            for (int r = 0; r < 6; ++r) {
                WAIT_VM0();
                const unsigned long long t0 = __builtin_amdgcn_s_memtime();
                bst4s(ws, ring + RING_CAL + p * 8192 + lane * 4, (unsigned)r);
                WAIT_VM0();
                const unsigned d = (unsigned)(__builtin_amdgcn_s_memtime() - t0);
                if (r > 0 && d < b) b = d;
            }
            best[p] = b; }
        if (lane == 0) { bst4s(ws, flags + FL_PAR, best[1] < best[0] ? 2u : 1u); bst4s(ws, flags + FL_PAR + 4, best[0]); bst4s(ws, flags + FL_PAR + 8, best[1]); }
    }
    const int q4 = n >> 2, p4 = n & 3;
    bf16x16 vfr[16], wo[4];
    {
        const u32x4 rq = mkrs(a.wq + h * 256, 0x7FFFFFFFu), rk = mkrs(a.wk + h * 256, 0x7FFFFFFFu), rv = mkrs(a.wv + h * 256, 0x7FFFFFFFu), ro = mkrs(a.wo + (size_t)h * 131072 + c * 256, 0x7FFFFFFFu);
        const int p = lane & 15, lg = lane >> 4, f = lg | (((w >> 1) & 1) << 2);
        const unsigned vw = (unsigned)lg * 8192u + (unsigned)((((p >> 1) ^ f) << 1) | (p & 1)) * 16u;
        const unsigned vo = (unsigned)lg * 1024u + (unsigned)((((p >> 1) ^ fo_rh(4 * w + lg)) << 1) | (p & 1)) * 16u;
        const unsigned l0 = sgpr(ldsoff(lds));
        auto issue = [&](int j) {
            const unsigned dst = l0 + (j & 3) * 32768 + w * 1024;
            const int row0 = j < 4 ? 128 * j : j < 8 ? 128 * (j - 4) : j < 10 ? 256 + 128 * (j - 8) : j < 12 ? 128 * (j - 10) : 0;
#pragma unroll
            for (int i = 0; i < 8; ++i) {
                if (j < 12) blds16(j < 4 ? rq : j < 8 ? rk : rv, vw, (unsigned)(row0 + 4 * w + 16 * i) * 8192u, dst + i * 4096);
                else blds16(ro, vo, (unsigned)(4 * w + 16 * i) * 1024u, dst + i * 4096);
            }
        };
        issue(0); issue(1); issue(2);
#pragma unroll
        for (int j = 0; j < 13; ++j) {
            if (j <= 10) asm volatile("s_waitcnt vmcnt(16) lgkmcnt(0)\n\ts_barrier" ::: "memory");
            else if (j == 11) asm volatile("s_waitcnt vmcnt(8) lgkmcnt(0)\n\ts_barrier" ::: "memory");
            else asm volatile("s_waitcnt vmcnt(0) lgkmcnt(0)\n\ts_barrier" ::: "memory");
            if (j + 3 < 13) issue(j + 3);
            const ldsp img = lds + (j & 3) * 32768;
            if (j < 8) {
                const unsigned b0 = ldsoff(w256_addr(img, 8 * g + q4, 2 * w, p4)), b1 = ldsoff(w256_addr(img, 8 * g + q4, 2 * w + 1, p4));
                if (j == 0) asm volatile(RH_LOADW_Q0 :: "v"(b0), "v"(b1) : "memory", RH_CLOB_Q0);
                if (j == 1) asm volatile(RH_LOADW_Q1 :: "v"(b0), "v"(b1) : "memory", RH_CLOB_Q1);
                if (j == 2) asm volatile(RH_LOADW_Q2 :: "v"(b0), "v"(b1) : "memory", RH_CLOB_Q2);
                if (j == 3) asm volatile(RH_LOADW_Q3 :: "v"(b0), "v"(b1) : "memory", RH_CLOB_Q3);
                if (j == 4) asm volatile(RH_LOADW_K0 :: "v"(b0), "v"(b1) : "memory", RH_CLOB_K0);
                if (j == 5) asm volatile(RH_LOADW_K1 :: "v"(b0), "v"(b1) : "memory", RH_CLOB_K1);
                if (j == 6) asm volatile(RH_LOADW_K2 :: "v"(b0), "v"(b1) : "memory", RH_CLOB_K2);
                if (j == 7) asm volatile(RH_LOADW_K3 :: "v"(b0), "v"(b1) : "memory", RH_CLOB_K3);
            } else if (j < 12) {
                const int f0 = j < 10 ? 8 + 4 * (j - 8) : 4 * (j - 10);
#pragma unroll
                for (int i = 0; i < 4; ++i) { vfr[f0 + i] = rh_wfrag(img, 64 * (i >> 1), 2 * w + (i & 1), g, q4, p4); PINV(vfr[f0 + i]); }
            } else {
#pragma unroll
                for (int T = 0; T < 2; ++T)
#pragma unroll
                    for (int ks = 0; ks < 2; ++ks) {
                        bf16x4 r[4];
#pragma unroll
                        for (int rr = 0; rr < 4; ++rr) {
                            const int kl0 = (rr < 2 ? 0 : 32) + 8 * g + 4 * (rr & 1);
                            const int e = 64 * ks + 16 * (kl0 >> 4) + 2 * ((kl0 >> 2) & 3) + (q4 & 1) + 8 * (q4 >> 1);
                            r[rr] = trread(img + e * 256 + (((2 * w + T) ^ fo_rh(e)) << 5) + p4 * 8);
                        }
                        wo[2 * T + ks] = cat8(cat4(r[0], r[1]), cat4(r[2], r[3])); PINV(wo[2 * T + ks]);
                    }
            }
        }
        WAIT_LGKM0(); __syncthreads();
#pragma unroll
        for (int i = 0; i < 8; ++i) {
            const bf16x16 fr = i < 4 ? vfr[i] : wo[i - 4];
            const ldsp d = lds + RH_WV + (i * 4 + w) * 2048 + lane * 16;
            *(LAS bf16x8*)d = __builtin_shufflevector(fr, fr, 0, 1, 2, 3, 4, 5, 6, 7);
            *(LAS bf16x8*)(d + 1024) = __builtin_shufflevector(fr, fr, 8, 9, 10, 11, 12, 13, 14, 15);
        }
    }
    TLMARK(a.ws, a.layer * 32 + 9);
    for (int k = tid; k < 512; k += 256) {
        unsigned short v[8];
#pragma unroll
        for (int i = 0; i < 8; ++i) v[i] = *(const GAS unsigned short*)(a.mem0 + (i * 512 + k) * 2);
        u32x4 x = {v[0] | ((unsigned)v[1] << 16), v[2] | ((unsigned)v[3] << 16), v[4] | ((unsigned)v[5] << 16), v[6] | ((unsigned)v[7] << 16)};
        ldsw128(lds + RH_MT + k * 16 + (k >> 4) * 128, x);
    }
    const unsigned ONE = 0x3F80u, OOR = 0x40000000u;
    u32x6 sel6; u32x4 selR;
    {
        const int d = (n & 7) - 4 * (g & 1);
        const unsigned lo = (d == 0 ? ONE : 0) | (d == 1 ? ONE << 16 : 0), hi = (d == 2 ? ONE : 0) | (d == 3 ? ONE << 16 : 0);
        sel6 = (u32x6){0, 0, lo, hi, 0, 0};
        const int j = n & 7;
        selR.x = (j == 0 ? ONE : 0) | (j == 1 ? ONE << 16 : 0); selR.y = (j == 2 ? ONE : 0) | (j == 3 ? ONE << 16 : 0);
        selR.z = (j == 4 ? ONE : 0) | (j == 5 ? ONE << 16 : 0); selR.w = (j == 6 ? ONE : 0) | (j == 7 ? ONE << 16 : 0);
    }
    const unsigned par = poll_flag_ge(ws, flags + FL_PAR, 1, dead) - 1u;
    const unsigned pofs = (par & 1) * 8192u;
    const unsigned pring = ring + RING_P + pofs;
    const unsigned pst = pring + (unsigned)(c * 8 + (h >> 2)) * 16384u + (h & 3) * 2048u + (unsigned)((4 * w + 2 * (g >> 1) + (n >> 3) + h) & 15) * 128u + (n & 7) * 16u + (g & 1) * 8u;
    unsigned pld[2];
#pragma unroll
    for (int i = 0; i < 2; ++i) { const int hp = 16 * i + (lane >> 2);
        pld[i] = pring + (unsigned)(c * 8 + (hp >> 2)) * 16384u + (hp & 3) * 2048u + (unsigned)(((h >> 1) + hp) & 15) * 128u + (h & 1) * 64u + (lane & 3) * 16u; }
    const unsigned mst = (n < 4 && g < 2) ? ring + RING_M + pofs + (unsigned)(128 * c + 4 * h + n) * 16u + g * 8u : OOR;
    const int kq = 128 * w + lane;
    const unsigned mld = ring + RING_M + pofs + kq * 16u;
    const unsigned mtw = ldsoff(lds + RH_MT + kq * 16 + (kq >> 4) * 128);
    const unsigned afp = ldsoff(lds + RH_MT + (16 * g + (q4 & 1) + 4 * (q4 >> 1) + 2 * (p4 >> 1)) * 16 + g * 128 + (p4 & 1) * 8);
    const unsigned wvlo = ldsoff(lds + RH_WV + w * 2048 + lane * 16);
    const unsigned kvoff = (unsigned)h * 8192u + w * 2048u + lane * 16u;
    const unsigned myflag = flags + (c ? FL_VM : FL_KM) + h * 256, h1f = flags + FL_H1 + h * 256;
    const unsigned flo = (c < 2 && tid == 0) ? myflag : OOR;
    const unsigned sxb = ldsoff(lds + RH_SX + lane * 16);
    unsigned h1seen = __builtin_amdgcn_readfirstlane(poll_flag_ge(ws, h1f, 1, dead));
    u32x8 kf2, vf2;
    { const u32x4 k0 = bld16s(ws, WS_KF + kvoff), k1 = bld16s(ws, WS_KF + kvoff + 1024), v0 = bld16s(ws, WS_VF + kvoff), v1 = bld16s(ws, WS_VF + kvoff + 1024);
      kf2 = __builtin_shufflevector(k0, k1, 0, 1, 2, 3, 4, 5, 6, 7); vf2 = __builtin_shufflevector(v0, v1, 0, 1, 2, 3, 4, 5, 6, 7); }
    WAIT_VM0(); __syncthreads();
    TLMARK(a.ws, a.layer * 32 + 10);
    const unsigned long long tl0 = __builtin_amdgcn_s_memtime(), tr0 = __builtin_amdgcn_s_memrealtime();
    __builtin_amdgcn_s_waitcnt(0xC07F);
    {
        const u32x4 ad0 = {afp, wvlo, sxb + w * 2048u, sxb + ((w + 1) & 3) * 2048u};
        const u32x4 ad1 = {sxb + ((w + 2) & 3) * 2048u, sxb + ((w + 3) & 3) * 2048u, ldsoff(lds + RH_OB + (n & 7) * 272 + (32 * w + 16 * hs + 4 * g) * 2), ldsoff(lds + RH_OB + (n & 7) * 272 + (16 * g + 8 * hs) * 2)};
        const u32x3 ad2 = {mtw, pst, w == 3 ? pld[0] : OOR};
        const unsigned mfb = (unsigned)h * 4096u + w * 1024u + lane * 16u;
        const u32x4 ad3 = {mst, mld, kvoff, c == 0 ? WS_KM + mfb : OOR};
        const u32x3 ad4 = {c == 1 ? WS_VM + mfb : OOR, flo, hs ? 0xEEEEu : 0x4444u};
        const unsigned long long wsp = (unsigned long long)a.ws;
        const u32x4 rs = {(unsigned)__builtin_amdgcn_readfirstlane((unsigned)wsp), (unsigned)__builtin_amdgcn_readfirstlane((unsigned)(wsp >> 32) & 0xFFFFu), WS_END, 0x00020000u};
        const unsigned long long mHS = __builtin_amdgcn_ballot_w64(hs != 0), mLM = __builtin_amdgcn_ballot_w64(lane < 32 && n < 8), mC = __builtin_amdgcn_ballot_w64(c == 0);
        const float cexp = 0.08838834764831845f * 1.4426950408889634f;
        const unsigned long long c2 = ((unsigned long long)__float_as_uint(cexp) << 32) | __float_as_uint(cexp);
        unsigned w1 = a.w1, w2 = a.w2;
        const unsigned sh1f = __builtin_amdgcn_readfirstlane(h1f), sdbg = __builtin_amdgcn_readfirstlane(WS_DBG + (1u << 20) - 65536u + ((c * 32 + h) * 4 + w) * 64u);
        asm volatile(RH_ASM
            : "+{v[152:159]}"(kf2), "+{v[160:167]}"(vf2), "+{s47}"(h1seen), "+{s45}"(w1), "+{s46}"(w2)
            : "{v[56:71]}"(cat16(vfr[4], vfr[5])), "{v[72:87]}"(cat16(vfr[6], vfr[7])), "{v[88:103]}"(cat16(vfr[8], vfr[9])), "{v[104:119]}"(cat16(vfr[10], vfr[11])),
              "{v[120:135]}"(cat16(vfr[12], vfr[13])), "{v[136:151]}"(cat16(vfr[14], vfr[15])),
              "{v[168:173]}"(sel6), "{v[174:177]}"(selR), "{v[232:235]}"(ad0), "{v[236:239]}"(ad1), "{v[240:242]}"(ad2), "{v[244:247]}"(ad3), "{v[248:250]}"(ad4),
              "{s[40:43]}"(rs), "{s48}"(sh1f), "{s49}"(sdbg), "{s[50:51]}"(mHS), "{s[54:55]}"(mLM), "{s65}"(w), "{s[72:73]}"(c2), "{s74}"(0x42700000u), "{s[76:77]}"(mC)
            : "memory", "vcc", "scc", "s44", RH_CLOB_S_NOW, RH_CLOB_V
            );
    }
    TLMARK(a.ws, a.layer * 32 + 11);
    if (a.dbg & 4) { const unsigned long long tl1 = __builtin_amdgcn_s_memtime(), tr1 = __builtin_amdgcn_s_memrealtime();
        if (threadIdx.x == 0) { const unsigned o = WS_DBG + (1u << 20) - 4096u + (c * 32 + h) * 16u; bst4s(ws, o, (unsigned)(tl1 - tl0)); bst4s(ws, o + 4, (unsigned)(tr1 - tr0)); bst4s(ws, o + 8, (unsigned)tr0); } }
}

DEV void stage_x_row(u32x4 rs, unsigned vbase, int s, unsigned dst, int w, int i) {
    blds16(rs, vbase ^ (unsigned)(i << 4), (unsigned)(s * 32 + 8 * w + i) * 1024u, dst + (8 * w + i) * 1024);
}
DEV void stage_x(u32x4 rs, unsigned vbase, int s, unsigned dst, int w) {
#pragma unroll
    for (int i = 0; i < 8; ++i) stage_x_row(rs, vbase, s, dst, w, i);
}
DEV void stage_stats(u32x4 rsw, int s, unsigned dst, int lane) {
    blds4s(rsw, (unsigned)lane * 4u, WS_STAT + (unsigned)s * 256u, dst);
}
DEV void wait_stats(rsrc_t ws, unsigned flags, int lane, bool& dead) {
    for (unsigned spin = 0;; ++spin) {
        MEMFENCE();
        const unsigned v = bld4s(ws, flags + FL_ST + (lane & 31) * 256);
        if (__builtin_amdgcn_ballot_w64(v == 0) == 0ull || dead) break;
        if (spin > SPIN_CAP) { dead = true; break; }
        __builtin_amdgcn_s_sleep(2);
    }
}
DEV float dot2(unsigned a, unsigned b, float c) { return __builtin_amdgcn_fdot2_f32_bf16(__builtin_bit_cast(bf16x2, a), __builtin_bit_cast(bf16x2, b), c, false); }
DEV void row_stats(ldsp x, ldsp stats, int tid) {
    const int row = tid >> 3, part = tid & 7;
    float s1 = 0.f, s2 = 0.f;
#pragma unroll
    for (int i = 0; i < 8; ++i) {
        const u32x4 v = ldsr128(x + row * 1024 + (part + 8 * ((i + row) & 7)) * 16);
        s1 = dot2(v.x, 0x3F803F80u, s1); s1 = dot2(v.y, 0x3F803F80u, s1); s1 = dot2(v.z, 0x3F803F80u, s1); s1 = dot2(v.w, 0x3F803F80u, s1);
        s2 = dot2(v.x, v.x, s2); s2 = dot2(v.y, v.y, s2); s2 = dot2(v.z, v.z, s2); s2 = dot2(v.w, v.w, s2);
    }
    s1 += __shfl_xor(s1, 1); s2 += __shfl_xor(s2, 1); s1 += __shfl_xor(s1, 2); s2 += __shfl_xor(s2, 2); s1 += __shfl_xor(s1, 4); s2 += __shfl_xor(s2, 4);
    const float mean = s1 * (1.f / 512.f), var = fmaxf(s2 * (1.f / 512.f) - mean * mean, 0.f);
    if (part == 0) *(LAS f32x2*)(stats + row * 8) = (f32x2){mean, __builtin_amdgcn_rsqf(var + 1e-5f)};
}
DEV bf16x8 xfrag(ldsp x, int tt, int ks, int n, int g) { const int t = 16 * tt + n; return asbf8(ldsr128(x + t * 1024 + (((4 * ks + g) ^ n) << 4))); }
DEV bf16x8 wfrag(ldsp base, int ks, int et, int g, int q4, int p4) {
    const int k0 = 32 * ks + 8 * g + q4;
    return cat4(trread(w256_addr(base, k0, et, p4)), trread(w256_addr(base, k0 + 4, et, p4)));
}
template <int NK> DEV float colsum(const bf16x8 (&f)[NK]) {
    float s = 0.f;
#pragma unroll
    for (int ks = 0; ks < NK; ++ks) { const u32x4 v = asu4(f[ks]); s = dot2(v.x, 0x3F803F80u, s); s = dot2(v.y, 0x3F803F80u, s); s = dot2(v.z, 0x3F803F80u, s); s = dot2(v.w, 0x3F803F80u, s); }
    return sum_xor32(sum_xor16(s));
}

constexpr int H1_X = 0  , H1_ST = 131072  , H1_CS = H1_ST + 2048;
DEV void role_h1(const BArgs& a, ldsp lds, int h) {
    const int tid = threadIdx.x, lane = tid & 63, w = __builtin_amdgcn_readfirstlane(tid >> 6);
    const int n = lane & 15, g = lane >> 4, q4 = n >> 2, p4 = n & 3;
    const rsrc_t ws = mkrsrc(a.ws, WS_END);
    const unsigned flags = WS_FLAGS + a.layer * 65536u;
    bf16x8 wk[2][16], wv[2][16];
    stage_w256(a.wk + h * 256, 8192, lds, 512, w, lane);
    WAIT_VM0(); __syncthreads();
#pragma unroll
    for (int t = 0; t < 2; ++t)
#pragma unroll
        for (int ks = 0; ks < 16; ++ks) wk[t][ks] = wfrag(lds, ks, 2 * w + t, g, q4, p4);
    { const float c0 = colsum(wk[0]), c1 = colsum(wk[1]);
      if (g == 0) { *(LAS float*)(lds + H1_CS + (32 * w + n) * 4) = c0; *(LAS float*)(lds + H1_CS + (32 * w + 16 + n) * 4) = c1; } }
#pragma unroll
    for (int t = 0; t < 2; ++t)
#pragma unroll
        for (int ks = 0; ks < 16; ++ks) PINA(wk[t][ks]);
    __syncthreads();
    stage_w256(a.wv + h * 256, 8192, lds, 512, w, lane);
    WAIT_VM0(); __syncthreads();
#pragma unroll
    for (int t = 0; t < 2; ++t)
#pragma unroll
        for (int ks = 0; ks < 16; ++ks) wv[t][ks] = wfrag(lds, ks, 2 * w + t, g, q4, p4);
    const float csv0 = colsum(wv[0]), csv1 = colsum(wv[1]);
#pragma unroll
    for (int t = 0; t < 2; ++t)
#pragma unroll
        for (int ks = 0; ks < 16; ++ks) PINA(wv[t][ks]);
    const f32x4 csk0 = *(LAS f32x4*)(lds + H1_CS + (32 * w + 4 * g) * 4), csk1 = *(LAS f32x4*)(lds + H1_CS + (32 * w + 16 + 4 * g) * 4);
    __syncthreads();
    TLMARK(a.ws, a.layer * 32 + 12);
    const char* xb = a.ws + WS_XB;
    bool dead = false;
    wait_stats(ws, flags, lane, dead);
    const u32x4 rsx = mkrs(xb, 2u << 20), rsw = mkrs(a.ws, WS_END);
    const unsigned xvb = (unsigned)(lane ^ (8 * (w & 1))) << 4, xl0 = sgpr(ldsoff(lds + H1_X)), sl0 = sgpr(ldsoff(lds + H1_ST + w * 256));
    stage_x(rsx, xvb, 0, xl0, w);
    stage_stats(rsw, 0, sl0, lane);
    WAIT_VM0();
#define HS(t)
    for (int s = 0; s < NSEG; ++s) {
        HS(t0_);
        const ldsp x = lds + H1_X + (s & 1) * 32768, stp = lds + H1_ST + (s & 1) * 1024 + w * 256;
        asm volatile("s_waitcnt vmcnt(4) lgkmcnt(0)\n\ts_barrier" ::: "memory");
        HS(t1_);
        const unsigned xn = xl0 + ((s + 1) & 1) * 32768;
        const int sn = s + 1 < NSEG ? s + 1 : s;
        f32x4 ka[2][2], va[2][2];
        bf16x8 xf[2][2][4];
#pragma unroll
        for (int j = 0; j < 4; ++j) { xf[0][0][j] = xfrag(x, 0, j, n, g); xf[0][1][j] = xfrag(x, 1, j, n, g); }
#pragma unroll
        for (int kb = 0; kb < 4; ++kb) {
            SBAR();
            if (kb < 3) {
#pragma unroll
                for (int j = 0; j < 4; ++j) { xf[(kb + 1) & 1][0][j] = xfrag(x, 0, 4 * kb + 4 + j, n, g); xf[(kb + 1) & 1][1][j] = xfrag(x, 1, 4 * kb + 4 + j, n, g); }
            }
            SBAR();
#pragma unroll
            for (int j = 0; j < 4; ++j) {
                const int ks = 4 * kb + j;
#pragma unroll
                for (int t = 0; t < 2; ++t) {
                    if (ks == 0) { MF0_AV(ka[t][0], wk[t][ks], xf[0][0][0]); MF0_AV(ka[t][1], wk[t][ks], xf[0][1][0]); MF0_VA(va[t][0], xf[0][0][0], wv[t][ks]); MF0_VA(va[t][1], xf[0][1][0], wv[t][ks]); }
                    else { MFA_AV(ka[t][0], wk[t][ks], xf[kb & 1][0][j]); MFA_AV(ka[t][1], wk[t][ks], xf[kb & 1][1][j]); MFA_VA(va[t][0], xf[kb & 1][0][j], wv[t][ks]); MFA_VA(va[t][1], xf[kb & 1][1][j], wv[t][ks]); }
                }
                if (kb < 2) stage_x_row(rsx, xvb, sn, xn, w, 4 * kb + j);
                if (kb == 2 && j == 0) stage_stats(rsw, sn, sl0 + ((s + 1) & 1) * 1024, lane);
                SBAR();
            }
        }
        HS(t2_);
        asm volatile("s_waitcnt vmcnt(9)\n\ts_barrier" ::: "memory");
        HS(t3_);
        if (s > 0 && tid == 0) bst4s(ws, flags + FL_H1 + h * 256, (unsigned)s);
        const unsigned o = (unsigned)(s * 32 + h) * 8192u + w * 2048u + lane * 16u;
        u32x4 st4[4];
#pragma unroll
        for (int tt = 0; tt < 2; ++tt) {
            const f32x2 st = *(LAS f32x2*)(stp + (16 * tt + n) * 8);
            float r[8];
#pragma unroll
            for (int j = 0; j < 4; ++j) { r[j] = fmaf(ka[0][tt][j], st[0], st[1] * csk0[j]); r[4 + j] = fmaf(ka[1][tt][j], st[0], st[1] * csk1[j]); }
            st4[tt] = (u32x4){pk2(r[0], r[1]), pk2(r[2], r[3]), pk2(r[4], r[5]), pk2(r[6], r[7])};
        }
        {
            const f32x4 sv0 = *(LAS f32x4*)(stp + g * 32), sv1 = *(LAS f32x4*)(stp + g * 32 + 16), sv2 = *(LAS f32x4*)(stp + 128 + g * 32), sv3 = *(LAS f32x4*)(stp + 128 + g * 32 + 16);
            const float rs[8] = {sv0[0], sv0[2], sv1[0], sv1[2], sv2[0], sv2[2], sv3[0], sv3[2]};
            const float nm[8] = {sv0[1], sv0[3], sv1[1], sv1[3], sv2[1], sv2[3], sv3[1], sv3[3]};
#pragma unroll
            for (int t = 0; t < 2; ++t) {
                const float cs = t ? csv1 : csv0;
                float r[8];
#pragma unroll
                for (int j = 0; j < 8; ++j) r[j] = fmaf(va[t][j >> 2][j & 3], rs[j], nm[j] * cs);
                st4[2 + t] = (u32x4){pk2(r[0], r[1]), pk2(r[2], r[3]), pk2(r[4], r[5]), pk2(r[6], r[7])};
            }
        }
        HS(t4_);
        MEMFENCE();
        bst16s(ws, WS_KF + o, st4[0]); bst16s(ws, WS_KF + o + 1024, st4[1]); bst16s(ws, WS_VF + o, st4[2]); bst16s(ws, WS_VF + o + 1024, st4[3]);
        MEMFENCE();
    }
    WAIT_VM0(); BAR();
    if (tid == 0) { bst4s(ws, flags + FL_H1 + h * 256, (unsigned)NSEG); if (dead) bst4s(ws, flags + FL_ABORT, 4u); }
    TLMARK(a.ws, a.layer * 32 + 14);
}

constexpr int H2_X = 0  , H2_QX = 65536  , H2_OX = H2_QX + 8192  , H2_ST = 131072  , H2_CS = H2_ST + 2048;
DEV void role_h2(const BArgs& a, ldsp lds, int h, int par) {
    const int tid = threadIdx.x, lane = tid & 63, w = __builtin_amdgcn_readfirstlane(tid >> 6);
    const int n = lane & 15, g = lane >> 4, q4 = n >> 2, p4 = n & 3;
    const rsrc_t ws = mkrsrc(a.ws, WS_END);
    const unsigned flags = WS_FLAGS + a.layer * 65536u;
    bool dead = false;
    bf16x8 wq[2][16], wo[8][4];
    stage_w256(a.wq + h * 256, 8192, lds, 512, w, lane);
    WAIT_VM0(); __syncthreads();
#pragma unroll
    for (int t = 0; t < 2; ++t)
#pragma unroll
        for (int ks = 0; ks < 16; ++ks) wq[t][ks] = wfrag(lds, ks, 2 * w + t, g, q4, p4);
    { const float c0 = colsum(wq[0]), c1 = colsum(wq[1]);
      if (g == 0) { *(LAS float*)(lds + H2_CS + (32 * w + n) * 4) = c0; *(LAS float*)(lds + H2_CS + (32 * w + 16 + n) * 4) = c1; } }
#pragma unroll
    for (int t = 0; t < 2; ++t)
#pragma unroll
        for (int ks = 0; ks < 16; ++ks) PINA(wq[t][ks]);
    __syncthreads();
    {   const u32x4 rso = mkrs(a.wo + (size_t)h * 131072, 0x7FFFFFFFu);
#pragma unroll
        for (int i = 0; i < 32; ++i) { const int e = 4 * i + w;
            blds16(rso, (unsigned)(((((lane >> 1) ^ (e & 7)) << 1) | (lane & 1)) << 4), (unsigned)e * 1024u, sgpr(ldsoff(lds)) + e * 1024); }
    }
    WAIT_VM0(); __syncthreads();
#pragma unroll
    for (int ct = 0; ct < 8; ++ct)
#pragma unroll
        for (int u = 0; u < 4; ++u) {
            const int e0 = 32 * u + 4 * g + q4, e1 = e0 + 16, sl = 8 * w + ct;
            wo[ct][u] = cat4(trread(lds + e0 * 1024 + ((sl ^ (e0 & 7)) << 5) + p4 * 8), trread(lds + e1 * 1024 + ((sl ^ (e1 & 7)) << 5) + p4 * 8));
            PINA(wo[ct][u]);
        }
    const f32x4 csq0 = *(LAS f32x4*)(lds + H2_CS + (32 * w + 4 * g) * 4), csq1 = *(LAS f32x4*)(lds + H2_CS + (32 * w + 16 + 4 * g) * 4);
    __syncthreads();
    const char* xb = a.ws + WS_XB;
    const float cexp = 0.08838834764831845f * 1.4426950408889634f;
    const int tt = w & 1, eh = w >> 1;
    wait_stats(ws, flags, lane, dead);
    const u32x4 rsx = mkrs(xb, 2u << 20), rsw = mkrs(a.ws, WS_END);
    const unsigned xvb = (unsigned)(lane ^ (8 * (w & 1))) << 4, xl0 = sgpr(ldsoff(lds + H2_X)), sl0 = sgpr(ldsoff(lds + H2_ST + w * 256));
    stage_x(rsx, xvb, par, xl0, w);
    stage_stats(rsw, par, sl0, lane);
    WAIT_VM0();
    unsigned seenH1 = 0, seenM = 0;
    int it = 0;
    for (int s = par; s < NSEG; s += 2, ++it) {
        const ldsp x = lds + H2_X + (it & 1) * 32768, stp = lds + H2_ST + (it & 1) * 1024 + w * 256;
        asm volatile("s_waitcnt vmcnt(8) lgkmcnt(0)\n\ts_barrier" ::: "memory");
        const unsigned xn = xl0 + ((it + 1) & 1) * 32768;
        const int sn = s + 2 < NSEG ? s + 2 : s;
        f32x4 qa[2][2];
        bf16x8 xf[2][2][4];
#pragma unroll
        for (int j = 0; j < 4; ++j) { xf[0][0][j] = xfrag(x, 0, j, n, g); xf[0][1][j] = xfrag(x, 1, j, n, g); }
#pragma unroll
        for (int kb = 0; kb < 4; ++kb) {
            SBAR();
            if (kb < 3) {
#pragma unroll
                for (int j = 0; j < 4; ++j) { xf[(kb + 1) & 1][0][j] = xfrag(x, 0, 4 * kb + 4 + j, n, g); xf[(kb + 1) & 1][1][j] = xfrag(x, 1, 4 * kb + 4 + j, n, g); }
            }
            SBAR();
#pragma unroll
            for (int j = 0; j < 4; ++j) {
                const int ks = 4 * kb + j;
#pragma unroll
                for (int t = 0; t < 2; ++t) {
                    if (ks == 0) { MF0_AV(qa[t][0], wq[t][ks], xf[0][0][0]); MF0_AV(qa[t][1], wq[t][ks], xf[0][1][0]); }
                    else { MFA_AV(qa[t][0], wq[t][ks], xf[kb & 1][0][j]); MFA_AV(qa[t][1], wq[t][ks], xf[kb & 1][1][j]); }
                }
                if (kb < 2) stage_x_row(rsx, xvb, sn, xn, w, 4 * kb + j);
                if (kb == 2 && j == 0) stage_stats(rsw, sn, sl0 + ((it + 1) & 1) * 1024, lane);
                SBAR();
            }
        }
        asm volatile("s_waitcnt vmcnt(9)\n\ts_barrier" ::: "memory");
        if (it > 0 && tid == 0) bst4s(ws, flags + FL_H2 + (par * 32 + h) * 256, (unsigned)it);
        if (seenH1 < (unsigned)(s + 1)) seenH1 = __builtin_amdgcn_readfirstlane(poll_flag_ge(ws, flags + FL_H1 + h * 256, s + 1, dead));
        if (seenM < (unsigned)(s + 1)) { const unsigned a1 = poll_flag_ge(ws, flags + FL_KM + h * 256, s + 1, dead), a2 = poll_flag_ge(ws, flags + FL_VM + h * 256, s + 1, dead); seenM = __builtin_amdgcn_readfirstlane(min(a1, a2)); }
        const unsigned o8 = (unsigned)(s * 32 + h) * 8192u + lane * 16u, o4 = (unsigned)(s * 32 + h) * 4096u + lane * 16u;
        u32x4 kf[4][2], km[4], vf[2][2], vm[2];
#pragma unroll
        for (int u = 0; u < 4; ++u) { kf[u][0] = bld16s(ws, WS_KF + o8 + u * 2048); kf[u][1] = bld16s(ws, WS_KF + o8 + u * 2048 + 1024); km[u] = bld16s(ws, WS_KM + o4 + u * 1024); }
#pragma unroll
        for (int u = 0; u < 2; ++u) { vf[u][0] = bld16s(ws, WS_VF + o8 + (2 * eh + u) * 2048); vf[u][1] = bld16s(ws, WS_VF + o8 + (2 * eh + u) * 2048 + 1024); vm[u] = bld16s(ws, WS_VM + o4 + (2 * eh + u) * 1024); }
        SBAR();
#pragma unroll
        for (int u = 0; u < 2; ++u) {
            const f32x2 st = *(LAS f32x2*)(stp + (16 * u + n) * 8);
            const float sc = st[0] * cexp, nm = st[1] * cexp;
            float r[8];
#pragma unroll
            for (int j = 0; j < 4; ++j) { r[j] = fmaf(qa[0][u][j], sc, nm * csq0[j]); r[4 + j] = fmaf(qa[1][u][j], sc, nm * csq1[j]); }
            ldsw128(lds + H2_QX + (u * 4 + w) * 1024 + lane * 16, (u32x4){pk2(r[0], r[1]), pk2(r[2], r[3]), pk2(r[4], r[5]), pk2(r[6], r[7])});
        }
        BAR();
        f32x4 s0, s1v, sm;
        u32x4 qf[4];
#pragma unroll
        for (int u = 0; u < 4; ++u) qf[u] = ldsr128(lds + H2_QX + (tt * 4 + u) * 1024 + lane * 16);
        SBAR();
        MF0N(s0, kf[0][0], qf[0]); MF0N(s1v, kf[0][1], qf[0]); MF0N(sm, km[0], qf[0]);
#pragma unroll
        for (int u = 1; u < 4; ++u) { MFA(s0, kf[u][0], qf[u]); MFA(s1v, kf[u][1], qf[u]); MFA(sm, km[u], qf[u]); }
        SBAR(); NOPS(7); SBAR();
        const int qi = 16 * tt + n;
#pragma unroll
        for (int r = 0; r < 4; ++r) { if (4 * g + r > qi) s0[r] = -INFINITY; if (16 + 4 * g + r > qi) s1v[r] = -INFINITY; }
        float mx = fmaxf(fmaxf(fmaxf(s0[0], s0[1]), fmaxf(s0[2], s0[3])), fmaxf(fmaxf(s1v[0], s1v[1]), fmaxf(s1v[2], s1v[3])));
        mx = fmaxf(mx, fmaxf(fmaxf(sm[0], sm[1]), fmaxf(sm[2], sm[3])));
        mx = max_xor32(max_xor16(mx));
        float p0[4], p1[4], pm[4];
#pragma unroll
        for (int r = 0; r < 4; ++r) { p0[r] = __builtin_amdgcn_exp2f(s0[r] - mx); p1[r] = __builtin_amdgcn_exp2f(s1v[r] - mx); pm[r] = __builtin_amdgcn_exp2f(sm[r] - mx); }
        float l = (p0[0] + p0[1]) + (p0[2] + p0[3]) + (p1[0] + p1[1]) + (p1[2] + p1[3]);
        if (g < 2) l += (pm[0] + pm[1]) + (pm[2] + pm[3]);
        l = sum_xor32(sum_xor16(l));
        const float rl = __builtin_amdgcn_rcpf(l);
        const u32x4 pseg = {pk2(p0[0], p0[1]), pk2(p0[2], p0[3]), pk2(p1[0], p1[1]), pk2(p1[2], p1[3])}, pmem = {pk2(pm[0], pm[1]), pk2(pm[2], pm[3]), 0, 0};
        f32x4 oa[2], ob[2];
        u32x4 vmA[2], vmB[2];
#pragma unroll
        for (int u = 0; u < 2; ++u) { vmA[u] = (u32x4){vm[u].x, vm[u].y, 0, 0}; vmB[u] = (u32x4){vm[u].z, vm[u].w, 0, 0}; }
        SBAR();
        MF0(oa[0], vf[0][0], pseg); MF0N(ob[0], vf[0][1], pseg); MF0N(oa[1], vf[1][0], pseg); MF0N(ob[1], vf[1][1], pseg);
        MFA(oa[0], vmA[0], pmem); MFA(ob[0], vmB[0], pmem); MFA(oa[1], vmA[1], pmem); MFA(ob[1], vmB[1], pmem);
        SBAR(); NOPS(7); SBAR();
#pragma unroll
        for (int u = 0; u < 2; ++u)
            ldsw128(lds + H2_OX + (tt * 4 + 2 * eh + u) * 1024 + lane * 16,
                    (u32x4){pk2(oa[u][0] * rl, oa[u][1] * rl), pk2(oa[u][2] * rl, oa[u][3] * rl), pk2(ob[u][0] * rl, ob[u][1] * rl), pk2(ob[u][2] * rl, ob[u][3] * rl)});
        BAR();
        bf16x8 of[2][4];
#pragma unroll
        for (int u2 = 0; u2 < 2; ++u2)
#pragma unroll
            for (int u = 0; u < 4; ++u) of[u2][u] = asbf8(ldsr128(lds + H2_OX + (u2 * 4 + u) * 1024 + lane * 16));
        const unsigned po = WS_P2 + (unsigned)(s * 32 + h) * 32768u + w * 8192u + lane * 16u;
        f32x4 cc[8][2];
        SBAR();
#pragma unroll
        for (int cp = 0; cp < 4; ++cp)
#pragma unroll
            for (int u2 = 0; u2 < 2; ++u2) {
                f32x4& c0 = cc[cp * 2 + u2][0]; f32x4& c1 = cc[cp * 2 + u2][1];
                MF0_AV(c0, wo[2 * cp][0], of[u2][0]); MF0_AV(c1, wo[2 * cp + 1][0], of[u2][0]);
#pragma unroll
                for (int u = 1; u < 4; ++u) { MFA_AV(c0, wo[2 * cp][u], of[u2][u]); MFA_AV(c1, wo[2 * cp + 1][u], of[u2][u]); }
            }
        SBAR(); NOPS(7); SBAR();
        MEMFENCE();
#pragma unroll
        for (int i = 0; i < 8; ++i) { const f32x4 c0 = cc[i][0], c1 = cc[i][1];
            bst16s(ws, po + i * 1024, (u32x4){pk2(c0[0], c0[1]), pk2(c0[2], c0[3]), pk2(c1[0], c1[1]), pk2(c1[2], c1[3])}); }
        MEMFENCE();
    }
    WAIT_VM0(); BAR();
    if (tid == 0) { bst4s(ws, flags + FL_H2 + (par * 32 + h) * 256, (unsigned)it); if (dead) bst4s(ws, flags + FL_ABORT, 2u); }
    TLMARK(a.ws, a.layer * 32 + 15);
}

DEV void role_r2(const BArgs& a, int r) {
    const int tid = threadIdx.x, lane = tid & 63, w = __builtin_amdgcn_readfirstlane(tid >> 6);
    const int n = lane & 15, g = lane >> 4;
    const rsrc_t ws = mkrsrc(a.ws, WS_END);
    const unsigned flags = WS_FLAGS + a.layer * 65536u;
    bool dead = false;
    const unsigned ONE = 0x3F80u;
    u32x4 a1, a2;
    {
        const int j = n - 8 * (g & 1);
        const u32x4 e = {(j == 0 ? ONE : 0) | (j == 1 ? ONE << 16 : 0), (j == 2 ? ONE : 0) | (j == 3 ? ONE << 16 : 0), (j == 4 ? ONE : 0) | (j == 5 ? ONE << 16 : 0), (j == 6 ? ONE : 0) | (j == 7 ? ONE << 16 : 0)};
        const u32x4 z = {0, 0, 0, 0};
        a1 = g < 2 ? e : z; a2 = g < 2 ? z : e;
    }
    {
        const char* xb = a.ws + WS_XB;
        u32x4 v[16];
#pragma unroll
        for (int i = 0; i < 16; ++i) v[i] = *(const GAS u32x4*)(xb + (size_t)(64 * r + 16 * w + i) * 1024 + lane * 16);
        float s1[16], s2[16];
#pragma unroll
        for (int i = 0; i < 16; ++i) {
            float a1 = 0.f, a2 = 0.f;
#pragma unroll
            for (int j = 0; j < 4; ++j) { const float lo = __uint_as_float(v[i][j] << 16), hi = __uint_as_float(v[i][j] & 0xFFFF0000u); a1 += lo + hi; a2 = fmaf(lo, lo, fmaf(hi, hi, a2)); }
            s1[i] = a1; s2[i] = a2;
        }
#pragma unroll
        for (int st = 0; st < 4; ++st) {
            const int half = 8 >> st, bit = 1 << st;
            const bool up = (lane & bit) != 0;
#pragma unroll
            for (int i = 0; i < half; ++i) {
                const float k1 = up ? s1[i + half] : s1[i], g1 = up ? s1[i] : s1[i + half], k2 = up ? s2[i + half] : s2[i], g2 = up ? s2[i] : s2[i + half];
                s1[i] = k1 + __shfl_xor(g1, bit); s2[i] = k2 + __shfl_xor(g2, bit);
            }
        }
        float t1 = sum_xor32(sum_xor16(s1[0])), t2 = sum_xor32(sum_xor16(s2[0]));
        const int row = ((lane & 1) << 3) | ((lane & 2) << 1) | ((lane & 4) >> 1) | ((lane & 8) >> 3);
        const float mean = t1 * (1.f / 512.f), var = fmaxf(t2 * (1.f / 512.f) - mean * mean, 0.f), rstd = __builtin_amdgcn_rsqf(var + 1e-5f);
        if (lane < 16) bst8s(ws, WS_STAT + (unsigned)(64 * r + 16 * w + row) * 8u, (u32x2){__float_as_uint(rstd), __float_as_uint(-mean * rstd)});
        WAIT_VM0(); __syncthreads();
        if (tid == 0) bst4s(ws, flags + FL_ST + r * 256, 1u);
        TLMARK(a.ws, a.layer * 32 + 17);
    }
    if (a.rolemask & 16) return;
    const int bw = r >> 3, cp = (r >> 1) & 3, tt = r & 1;
    const int col0 = 128 * bw + 32 * cp + 16 * (g & 1) + 4 * (g >> 1);
    for (int s = w; s < NSEG; s += 4) {
        {
            const unsigned want = (unsigned)(s >> 1) + 1u;
            for (unsigned spin = 0;; ++spin) {
                MEMFENCE();
                const unsigned v = bld4s(ws, flags + FL_H2 + ((s & 1) * 32 + (lane & 31)) * 256);
                if (__all(v >= want) || dead) break;
                if (spin > SPIN_CAP) { dead = true; break; }
                __builtin_amdgcn_s_sleep(4);
            }
        }
        f32x4 d1 = {0, 0, 0, 0}, d2 = d1;
        PINV(d1); PINV(d2);
        const unsigned po = WS_P2 + (unsigned)s * 32u * 32768u + r * 1024u + lane * 16u;
#pragma unroll
        for (int hb = 0; hb < 32; hb += 16) {
            u32x4 v[16];
#pragma unroll
            for (int i = 0; i < 16; ++i) v[i] = bld16s(ws, po + (hb + i) * 32768u);
#pragma unroll
            for (int i = 0; i < 16; ++i) { MFA1(d1, a1, v[i]); MFA(d2, a2, v[i]); }
        }
        SBAR(); NOPS(7); SBAR();
        const int tok = s * 32 + 16 * tt + n;
        const unsigned xo = (unsigned)tok * 512u + col0;
        const u32x4 r1 = bld16p(ws, WS_XF + xo * 4), r2 = bld16p(ws, WS_XF + (xo + 8) * 4);
#pragma unroll
        for (int j = 0; j < 4; ++j) { d1[j] += __uint_as_float(r1[j]); d2[j] += __uint_as_float(r2[j]); }
        const u32x2 b1 = {pk2(d1[0], d1[1]), pk2(d1[2], d1[3])}, b2 = {pk2(d2[0], d2[1]), pk2(d2[2], d2[3])};
        if (a.out) {
            *(GAS u32x2*)(a.out + xo * 2) = b1; *(GAS u32x2*)(a.out + (xo + 8) * 2) = b2;
        } else {
            bst16p(ws, WS_XF + xo * 4, __builtin_bit_cast(u32x4, d1)); bst16p(ws, WS_XF + (xo + 8) * 4, __builtin_bit_cast(u32x4, d2));
            bst8p(ws, WS_XB + xo * 2, b1); bst8p(ws, WS_XB + (xo + 8) * 2, b2);
        }
    }
    if (dead && lane == 0) bst4s(ws, flags + FL_ABORT, 3u);
    TLMARK(a.ws, a.layer * 32 + 16);
}

__global__ void __launch_bounds__(256, 1) kernelB(BArgs a) {
    __shared__ __attribute__((aligned(16))) char lds_raw[RH_END > 134144 ? RH_END : 134144];
    const ldsp lds = (ldsp)lds_raw;
    const int x = blockIdx.x & 7, y = blockIdx.x >> 3;
    if (x < 4) { if (a.rolemask & 2) role_rh(a, lds, 8 * x + (y >> 2), y & 3); }
    else {
        const int xq = x - 4;
        if (y < 8) { if (a.rolemask & 1) role_h1(a, lds, xq * 8 + y); }
        else if (y < 24) { if (a.rolemask & 4) role_h2(a, lds, xq * 8 + ((y - 8) >> 1), (y - 8) & 1); }
        else { if (a.rolemask & 8) role_r2(a, xq * 8 + (y - 24)); }
    }
}

struct AArgs {
    const char* xb;
    const char* xres;
    const char* wqkv; const char* wo;
    char* ws;
    int layer; int pad_;
};
constexpr float A_QSCALE = 0.25f * 1.4426950408889634f;

constexpr int K1_STAGE = 32768, K1_ST = 131072;
struct K1Stage { u32x4 rsx, rsw; unsigned xv, wvA, wvB; };
DEV void k1_stage_one(const K1Stage& k, unsigned buf, int tb, int c, int w, int i) {
    if (i < 4) blds16(k.rsx, k.xv, (unsigned)(tb * 128 + (4 * w + i) * 8) * 1024u + c * 128, buf + (4 * w + i) * 1024);
    else { const int j = i - 4; blds16(k.rsw, ((j >> 1) & 1) ? k.wvB : k.wvA, (unsigned)(c * 64 + (4 * w + j) * 4) * 3072u, buf + 16384 + (4 * w + j) * 1024); }
}
__global__ void __launch_bounds__(256, 1) kernelA1(AArgs a) {
    __shared__ __attribute__((aligned(16))) char lds_raw[K1_ST + 4096];
    const ldsp lds = (ldsp)lds_raw;
    const int tid = threadIdx.x, lane = tid & 63, w = __builtin_amdgcn_readfirstlane(tid >> 6);
    const int n = lane & 15, g = lane >> 4, q4 = n >> 2, p4 = n & 3;
    const int b = blockIdx.x, tb = (b & 3) * 4 + ((b >> 3) & 3), hp = ((b >> 2) & 1) * 8 + (b >> 5);
    const rsrc_t ws = mkrsrc(a.ws, WS_END);
    TLMARK(a.ws, a.layer * 32 + 0);
    K1Stage stg;
    {   stg.rsx = mkrs(a.xb, 2u << 20); stg.rsw = mkrs(a.wqkv, 512u * 3072u);
        const int t8 = lane >> 3, p = lane & 7;
        stg.xv = (unsigned)t8 * 1024u + (unsigned)((p ^ t8) << 4);
        const int pw = lane & 15, lg = lane >> 4;
        int sa = (pw >> 1) ^ lg, sb = (pw >> 1) ^ (lg | 4); sa = sa > 5 ? 5 : sa; sb = sb > 5 ? 5 : sb;
        stg.wvA = (unsigned)lg * 3072u + (sa >> 1) * 1024u + hp * 64u + (sa & 1) * 32u + (pw & 1) * 16u;
        stg.wvB = (unsigned)lg * 3072u + (sb >> 1) * 1024u + hp * 64u + (sb & 1) * 32u + (pw & 1) * 16u;
    }
    const unsigned l0 = sgpr(ldsoff(lds));
#pragma unroll
    for (int c = 0; c < 3; ++c)
#pragma unroll
        for (int i = 0; i < 8; ++i) k1_stage_one(stg, l0 + c * K1_STAGE, tb, c, w, i);
    f32x4 acc[2][6], sx[2], gr[2], cw0, cw1;
    {   const f32x4 z = {0, 0, 0, 0};
#pragma unroll
        for (int tt = 0; tt < 2; ++tt) { sx[tt] = z; gr[tt] = z; PINV(sx[tt]); PINV(gr[tt]);
#pragma unroll
            for (int j = 0; j < 6; ++j) { acc[tt][j] = z; PINV(acc[tt][j]); } }
        cw0 = z; cw1 = z; PINV(cw0); PINV(cw1); }
    u32x4 ones = {0x3F803F80u, 0x3F803F80u, 0x3F803F80u, 0x3F803F80u}; PINV(ones);
    const int wa_t = w, wb_t = 4 + (w & 1);
#pragma unroll 1
    for (int c = 0; c < 8; ++c) {
        if (c < 6) asm volatile("s_waitcnt vmcnt(16) lgkmcnt(0)\n\ts_barrier" ::: "memory"); else if (c == 6) asm volatile("s_waitcnt vmcnt(8) lgkmcnt(0)\n\ts_barrier" ::: "memory");
        else asm volatile("s_waitcnt vmcnt(0) lgkmcnt(0)\n\ts_barrier" ::: "memory");
        if (c == 0) TLMARK(a.ws, a.layer * 32 + 20);
        if (c == 1) TLMARK(a.ws, a.layer * 32 + 21);
        const ldsp xs = lds + (c & 3) * K1_STAGE, wsb = xs + 16384;
        bf16x8 xf[2][2], wf[2][6], wa[2], wb[2];
#pragma unroll
        for (int ks = 0; ks < 2; ++ks) {
#pragma unroll
            for (int tt = 0; tt < 2; ++tt) { const int t = 32 * w + 16 * tt + n; xf[ks][tt] = asbf8(ldsr128(xs + t * 128 + (((4 * ks + g) ^ (n & 7)) << 4))); }
#pragma unroll
            for (int j = 0; j < 6; ++j) wf[ks][j] = wfrag(wsb, ks, j, g, q4, p4);
            wa[ks] = wfrag(wsb, ks, wa_t, g, q4, p4); wb[ks] = wfrag(wsb, ks, wb_t, g, q4, p4);
        }
        SBAR();
        if (c + 3 < 8) {
#pragma unroll
            for (int i = 0; i < 8; ++i) k1_stage_one(stg, l0 + ((c + 3) & 3) * K1_STAGE, tb, c + 3, w, i);
        }
        SBAR();
#pragma unroll
        for (int ks = 0; ks < 2; ++ks) {
#pragma unroll
            for (int tt = 0; tt < 2; ++tt) {
#pragma unroll
                for (int j = 0; j < 4; ++j) MFA(acc[tt][j], wf[ks][j], xf[ks][tt]);
#pragma unroll
                for (int j = 4; j < 6; ++j) MFA(acc[tt][j], xf[ks][tt], wf[ks][j]);
                MFA(sx[tt], ones, xf[ks][tt]);
                MFA(gr[tt], xf[ks][tt], xf[ks][tt]);
            }
            MFA(cw0, wa[ks], ones);
            MFA(cw1, ones, wb[ks]);
        }
        SBAR();
    }
    NOPS(7);
    TLMARK(a.ws, a.layer * 32 + 22);
    float rs[2], nm[2];
#pragma unroll
    for (int tt = 0; tt < 2; ++tt) {
        const int r = n & 3;
        float dg = r == 0 ? gr[tt][0] : r == 1 ? gr[tt][1] : r == 2 ? gr[tt][2] : gr[tt][3];
        dg = __shfl(dg, n + 16 * (n >> 2));
        const float mean = sx[tt][0] * (1.f / 512.f), var = fmaxf(dg * (1.f / 512.f) - mean * mean, 0.f);
        rs[tt] = __builtin_amdgcn_rsqf(var + 1e-5f); nm[tt] = -mean * rs[tt];
        if (g == 0) *(LAS f32x2*)(lds + K1_ST + (32 * w + 16 * tt + n) * 8) = (f32x2){rs[tt], nm[tt]};
    }
    if (n == 0) *(LAS f32x4*)(lds + K1_ST + 1024 + (w * 16 + 4 * g) * 4) = cw0;
    if (w < 2 && g == 0) *(LAS float*)(lds + K1_ST + 1024 + ((4 + w) * 16 + n) * 4) = cw1[0];
    __syncthreads();
#pragma unroll
    for (int tt = 0; tt < 2; ++tt) {
        const int tok = tb * 128 + 32 * w + 16 * tt + n;
#pragma unroll
        for (int hh = 0; hh < 2; ++hh) {
            const int h = 2 * hp + hh;
            const f32x4 cq = *(LAS f32x4*)(lds + K1_ST + 1024 + (hh * 16 + 4 * g) * 4), ck = *(LAS f32x4*)(lds + K1_ST + 1024 + ((2 + hh) * 16 + 4 * g) * 4);
            float q[4], k[4];
#pragma unroll
            for (int r = 0; r < 4; ++r) { q[r] = fmaf(acc[tt][hh][r], rs[tt], nm[tt] * cq[r]) * A_QSCALE; k[r] = fmaf(acc[tt][2 + hh][r], rs[tt], nm[tt] * ck[r]); }
            bst8p(ws, WS_QA + (unsigned)(h * 2048 + tok) * 32u + g * 8u, (u32x2){pk2(q[0], q[1]), pk2(q[2], q[3])});
            const u32x2 kp = {pk2(k[0], k[1]), pk2(k[2], k[3])};
            bst8p(ws, WS_KA + (unsigned)h * 65536u + (tok >> 5) * 1024u + (g >> 1) * 512u + (tok & 31) * 16u + (g & 1) * 8u, kp);
            const float k0 = __uint_as_float(kp.x << 16), k1 = __uint_as_float(kp.x & 0xFFFF0000u), k2 = __uint_as_float(kp.y << 16), k3 = __uint_as_float(kp.y & 0xFFFF0000u);
            float nn = sum_xor32(sum_xor16(k0 * k0 + k1 * k1 + k2 * k2 + k3 * k3));
#pragma unroll
            for (int m = 1; m < 16; m <<= 1) nn = fmaxf(nn, __shfl_xor(nn, m));
            if (lane == 0) __builtin_amdgcn_raw_buffer_store_b32(__float_as_uint(nn), ws, WS_KN + (unsigned)(h * 128 + (tok >> 4)) * 4u, 0, 0);
            const f32x4 sa = *(LAS f32x4*)(lds + K1_ST + (32 * w + 16 * tt + 4 * g) * 8), sb = *(LAS f32x4*)(lds + K1_ST + (32 * w + 16 * tt + 4 * g + 2) * 8);
            const float cv = *(LAS float*)(lds + K1_ST + 1024 + ((4 + hh) * 16 + n) * 4);
            const float v0 = fmaf(acc[tt][4 + hh][0], sa[0], sa[1] * cv), v1 = fmaf(acc[tt][4 + hh][1], sa[2], sa[3] * cv), v2 = fmaf(acc[tt][4 + hh][2], sb[0], sb[1] * cv), v3 = fmaf(acc[tt][4 + hh][3], sb[2], sb[3] * cv);
            const int s16 = (tb * 128 + 32 * w + 16 * tt) >> 4;
            bst8p(ws, WS_VA + (unsigned)h * 65536u + s16 * 512u + (g & 1) * 256u + n * 16u + (g >> 1) * 8u, (u32x2){pk2(v0, v1), pk2(v2, v3)});
        }
    }
    TLMARK(a.ws, a.layer * 32 + 1);
}

constexpr int K2_K = 0, K2_V = 65536, K2_C = 131072, K2_END = K2_C + 4096;
DEV u32x4 pack8(const f32x16& p, int o) { return (u32x4){pk2(p[o], p[o + 1]), pk2(p[o + 2], p[o + 3]), pk2(p[o + 4], p[o + 5]), pk2(p[o + 6], p[o + 7])}; }
__global__ void __launch_bounds__(512, 1) kernelA2(AArgs a) {
    __shared__ __attribute__((aligned(16))) char lds_raw[K2_END];
    const ldsp lds = (ldsp)lds_raw;
    const int tid = threadIdx.x, lane = tid & 63, w = __builtin_amdgcn_readfirstlane(tid >> 6);
    const int b = blockIdx.x, h = (b & 7) * 4 + ((b >> 3) & 3), qb = b >> 5;
    const int q = lane & 31, hf = lane >> 5;
    TLMARK(a.ws, a.layer * 32 + 2);
    const char* kimg = a.ws + WS_KA + (size_t)h * 65536; const char* vimg = a.ws + WS_VA + (size_t)h * 65536;
    {   const u32x4 rk = mkrs(kimg, 65536), rv = mkrs(vimg, 65536); const unsigned l0 = sgpr(ldsoff(lds));
#pragma unroll
        for (int i = 0; i < 8; ++i) blds16(rk, lane * 16u, (w * 8 + i) * 1024u, l0 + K2_K + (w * 8 + i) * 1024);
#pragma unroll
        for (int i = 0; i < 8; ++i) blds16(rv, lane * 16u, (w * 8 + i) * 1024u, l0 + K2_V + (w * 8 + i) * 1024); }
    if (tid < 32) { const int s = tid >> 2, hh = (tid >> 1) & 1, z = tid & 1; const unsigned v = z ? 0u : 0x3F803F80u; ldsw128(lds + K2_C + s * 512 + hh * 256 + z * 16, (u32x4){v, v, v, v}); }
    const int tok = qb * 256 + w * 32 + q;
    const u32x4 qv = *(const GAS u32x4*)(a.ws + WS_QA + (size_t)(h * 2048 + tok) * 32 + hf * 16);
    float kn = 0.f;
    { const float* kp = (const float*)(a.ws + WS_KN) + h * 128; kn = fmaxf(kp[lane], kp[64 + lane]);
#pragma unroll
      for (int m = 1; m < 64; m <<= 1) kn = fmaxf(kn, __shfl_xor(kn, m)); }
    float qn = 0.f;
#pragma unroll
    for (int e = 0; e < 4; ++e) { const float lo = __uint_as_float(qv[e] << 16), hi = __uint_as_float(qv[e] & 0xFFFF0000u); qn = fmaf(lo, lo, fmaf(hi, hi, qn)); }
    qn = sum_xor32(qn);
    const float bound = sqrtf(qn) * sqrtf(kn) * 1.002f;
    const bf16x8 qf = asbf8(qv);
    f32x16 negm;
#pragma unroll
    for (int r = 0; r < 16; ++r) negm[r] = -bound;
    const ldsp kp = lds + K2_K + lane * 16;
    const int m = lane & 31;
    unsigned vaddr = (unsigned)(size_t)(m < 16 ? lds + K2_V + hf * 256 + m * 16 : lds + K2_C + hf * 256 + (m == 16 ? 0 : 16));
    const unsigned vinc = m < 16 ? 4096u : 0u;
    WAIT_VM0(); __syncthreads();
    TLMARK(a.ws, a.layer * 32 + 3);
    f32x16 o = {};
#pragma unroll 1
    for (int t4 = 0; t4 < 16; ++t4) {
#pragma unroll
        for (int i = 0; i < 4; ++i) {
            const bf16x8 kf = asbf8(ldsr128(kp + (t4 * 4 + i) * 1024));
            const bf16x8 v0 = *(LAS bf16x8*)(size_t)(vaddr + i * 1024), v1 = *(LAS bf16x8*)(size_t)(vaddr + i * 1024 + 512);
            f32x16 s = mfma32(kf, qf, negm);
#pragma unroll
            for (int r = 0; r < 16; ++r) s[r] = __builtin_amdgcn_exp2f(s[r]);
            o = mfma32(v0, asbf8(pack8(s, 0)), o);
            o = mfma32(v1, asbf8(pack8(s, 8)), o);
        }
        vaddr += vinc;
    }
    float l = __shfl(o[8], q);
    if (__any(!(l >= 5.4e-20f))) {
        float mrun = -INFINITY; o = (f32x16){};
        unsigned va2 = (unsigned)(size_t)(m < 16 ? lds + K2_V + hf * 256 + m * 16 : lds + K2_C + hf * 256 + (m == 16 ? 0 : 16));
        const f32x16 z = {};
#pragma unroll 1
        for (int t = 0; t < 64; ++t) {
            const bf16x8 kf = asbf8(ldsr128(kp + t * 1024));
            const unsigned vo = m < 16 ? (unsigned)t * 1024u : (unsigned)(t & 3) * 1024u;
            const bf16x8 v0 = *(LAS bf16x8*)(size_t)(va2 + vo), v1 = *(LAS bf16x8*)(size_t)(va2 + vo + 512);
            f32x16 s = mfma32(kf, qf, z);
            float mx = s[0];
#pragma unroll
            for (int r = 1; r < 16; ++r) mx = fmaxf(mx, s[r]);
            mx = fmaxf(max_xor32(mx), mrun);
            const float alpha = __builtin_amdgcn_exp2f(mrun - mx);
            mrun = mx;
#pragma unroll
            for (int r = 0; r < 16; ++r) { o[r] *= alpha; s[r] = __builtin_amdgcn_exp2f(s[r] - mx); }
            o = mfma32(v0, asbf8(pack8(s, 0)), o);
            o = mfma32(v1, asbf8(pack8(s, 8)), o);
        }
        l = __shfl(o[8], q);
    }
    const float rl = 1.0f / l;
    unsigned a0 = pk2(o[0] * rl, o[1] * rl), a1 = pk2(o[2] * rl, o[3] * rl), b0 = pk2(o[4] * rl, o[5] * rl), b1 = pk2(o[6] * rl, o[7] * rl);
    { auto r0 = __builtin_amdgcn_permlane32_swap(a0, b0, false, false); auto r1 = __builtin_amdgcn_permlane32_swap(a1, b1, false, false);
      *(GAS u32x4*)(a.ws + WS_OA + (size_t)tok * 1024 + h * 32 + hf * 16) = (u32x4){r0[0], r1[0], r0[1], r1[1]}; }
    TLMARK(a.ws, a.layer * 32 + 4);
}

constexpr int K3_X = 0, K3_W = 65536, K3_END = 131072;
__global__ void __launch_bounds__(256, 1) kernelA3(AArgs a) {
    __shared__ __attribute__((aligned(16))) char lds_raw[K3_END];
    const ldsp lds = (ldsp)lds_raw;
    const int tid = threadIdx.x, lane = tid & 63, w = __builtin_amdgcn_readfirstlane(tid >> 6);
    const int n = lane & 15, g = lane >> 4, q4 = n >> 2, p4 = n & 3;
    const int b = blockIdx.x, cb = (b >> 3) & 7, tb = (b & 7) * 4 + (b >> 6);
    const rsrc_t ws = mkrsrc(a.ws, WS_END);
    TLMARK(a.ws, a.layer * 32 + 5);
    const char* oa = a.ws + WS_OA;
    {   const u32x4 rx = mkrs(oa, 2u << 20), rw = mkrs(a.wo, 512u * 1024u); const unsigned l0 = sgpr(ldsoff(lds));
#pragma unroll
        for (int i = 0; i < 16; ++i) blds16(rx, (unsigned)(lane ^ i) << 4, (unsigned)(tb * 64 + w * 16 + i) * 1024u, l0 + K3_X + (w * 16 + i) * 1024);
        const int pos = (lane >> 1) & 7, half = lane & 1, kl = 2 * (lane >> 4) + (pos >> 2);
        const unsigned va = (unsigned)kl * 1024u + cb * 128u + (unsigned)((pos & 3) ^ (kl & 2)) * 32u + half * 16u, vb = (unsigned)kl * 1024u + cb * 128u + (unsigned)((pos & 3) ^ ((kl & 2) | 1)) * 32u + half * 16u;
#pragma unroll
        for (int i = 0; i < 16; ++i) blds16(rw, (i & 1) ? vb : va, (unsigned)(w * 16 + i) * 8192u, l0 + K3_W + (w * 16 + i) * 1024);
    }
    {
        const unsigned ring = WS_RING + a.layer * (2u << 20), flags = WS_FLAGS + a.layer * 65536u;
        const u32x4 s = {0xFFFFFFFFu, 0xFFFFFFFFu, 0xFFFFFFFFu, 0xFFFFFFFFu};
        bst16p(ws, ring + RING_P + (unsigned)b * 4096u + tid * 16u, s);
        if (b < 16) bst16p(ws, ring + RING_M + (unsigned)b * 4096u + tid * 16u, s);
        if (b >= 16 && b < 32) bst16p(ws, flags + (unsigned)(b - 16) * 4096u + tid * 16u, (u32x4){0, 0, 0, 0});
    }
    const int tw = w >> 1, cw = w & 1;
    const int tok0 = tb * 64 + 32 * tw, col0 = cb * 64 + 32 * cw;
    f32x4 res[2][2];
#pragma unroll
    for (int tt = 0; tt < 2; ++tt)
#pragma unroll
        for (int ct = 0; ct < 2; ++ct) {
            const size_t e = (size_t)(tok0 + 16 * tt + n) * 512 + col0 + 16 * ct + 4 * g;
            if (a.layer == 0) { const u32x2 v = *(const GAS u32x2*)(a.xres + e * 2);
                res[tt][ct] = (f32x4){__uint_as_float(v.x << 16), __uint_as_float(v.x & 0xFFFF0000u), __uint_as_float(v.y << 16), __uint_as_float(v.y & 0xFFFF0000u)}; }
            else res[tt][ct] = *(const GAS f32x4*)(a.xres + e * 4);
        }
    TLMARK(a.ws, a.layer * 32 + 23);
    WAIT_VM0(); __syncthreads();
    TLMARK(a.ws, a.layer * 32 + 24);
#pragma unroll
    for (int ks = 0; ks < 16; ++ks) {
        bf16x8 xf[2], wf[2];
#pragma unroll
        for (int tt = 0; tt < 2; ++tt) { const int t = 32 * tw + 16 * tt + n; xf[tt] = asbf8(ldsr128(lds + K3_X + t * 1024 + (((4 * ks + g) ^ n) << 4))); }
#pragma unroll
        for (int ct = 0; ct < 2; ++ct) {
            bf16x4 r[2];
#pragma unroll
            for (int j = 0; j < 2; ++j) { const int k = 32 * ks + 8 * g + q4 + 4 * j, u = ((k >> 3) & 1) | (k & 2);
                r[j] = trread(lds + K3_W + (k >> 1) * 256 + (((k & 1) * 4 + ((2 * cw + ct) ^ u)) << 5) + p4 * 8); }
            wf[ct] = cat4(r[0], r[1]);
        }
#pragma unroll
        for (int tt = 0; tt < 2; ++tt)
#pragma unroll
            for (int ct = 0; ct < 2; ++ct) res[tt][ct] = mfma16(wf[ct], xf[tt], res[tt][ct]);
    }
    TLMARK(a.ws, a.layer * 32 + 25);
#pragma unroll
    for (int tt = 0; tt < 2; ++tt)
#pragma unroll
        for (int ct = 0; ct < 2; ++ct) {
            const unsigned e = (unsigned)(tok0 + 16 * tt + n) * 512u + col0 + 16 * ct + 4 * g;
            const f32x4 v = res[tt][ct];
            bst16p(ws, WS_XF + e * 4, __builtin_bit_cast(u32x4, v));
            bst8p(ws, WS_XB + e * 2, (u32x2){pk2(v[0], v[1]), pk2(v[2], v[3])});
        }
    TLMARK(a.ws, a.layer * 32 + 6);
}

constexpr int W1_DEFAULT = 13, W2_DEFAULT = 14;
extern "C" void kernel_launch(void* const* d_in, const int* in_sizes, int n_in, void* d_out, int out_size, void* d_ws, size_t ws_size, hipStream_t stream) {
    char* ws = (char*)(((size_t)d_ws + ((size_t)2 << 20) - 1) & ~(((size_t)2 << 20) - 1));
    for (int layer = 0; layer < 2; ++layer) {
        AArgs a; a.xb = layer == 0 ? (const char*)d_in[0] : ws + WS_XB; a.xres = layer == 0 ? (const char*)d_in[0] : ws + WS_XF;
        a.wqkv = (const char*)d_in[1]; a.wo = (const char*)d_in[2]; a.ws = ws; a.layer = layer; a.pad_ = 0;
        hipLaunchKernelGGL(kernelA1, dim3(256), dim3(256), 0, stream, a);
        hipLaunchKernelGGL(kernelA2, dim3(256), dim3(512), 0, stream, a);
        hipLaunchKernelGGL(kernelA3, dim3(256), dim3(256), 0, stream, a);
        BArgs bb; bb.wq = (const char*)d_in[5]; bb.wk = (const char*)d_in[6]; bb.wv = (const char*)d_in[7]; bb.wo = (const char*)d_in[8]; bb.mem0 = (const char*)d_in[4];
        bb.ws = ws; bb.out = layer == 1 ? (char*)d_out : nullptr; bb.layer = layer; bb.rolemask = 15; bb.w1 = W1_DEFAULT; bb.w2 = W2_DEFAULT; bb.dbg = 0; bb.pad_ = 0;
        hipLaunchKernelGGL(kernelB, dim3(256), dim3(256), 0, stream, bb);
    }
}
```
